```python
import torch

def _softmax(x, axis=-1):
    return torch.softmax(x, dim=axis)
def _gelu(x):
    return torch.nn.functional.gelu(x, approximate="tanh")
def _silu(x):
    return torch.nn.functional.silu(x)
def _sigmoid(x):
    return torch.sigmoid(x)
def _layernorm(x, axis=-1, eps=1e-5):
    m = x.mean(dim=axis, keepdim=True)
    v = x.var(dim=axis, keepdim=True, unbiased=False)
    return (x - m) / torch.sqrt(v + eps)
def _scatter_add(tgt, idx, src, axis=0):
    return tgt.clone().index_add_(axis, idx, src)
def _segment_sum(src, seg, n):
    out = torch.zeros((n,) + src.shape[1:], dtype=src.dtype)
    return out.index_add_(0, seg, src)
def _pad(x, pw):
    flat = tuple(v for pair in reversed(pw) for v in pair)
    return torch.nn.functional.pad(x, flat)

kernel_name = 'standalone__gqa_block_cached__decode'

def setup_inputs():

    _w = (lambda a: (lambda t: t.to(torch.bfloat16) if t.is_floating_point() else t)(torch.as_tensor(a)))
    B, Sq, D = 1, 1, 448
    Hq, Hkv, Sk, Dh = 16, 8, 8192, 256
    assert Hq % Hkv == 0, (Hq, Hkv)
    return {
        "x": _w((lambda *s: torch.randn(*s, dtype=torch.bfloat16) * 0.8)(B, Sq, D)),
        "k_cache": _w((lambda *s: torch.randn(*s, dtype=torch.bfloat16) * 0.8)(B, Hkv, Sk, Dh)),
        "v_cache": _w((lambda *s: torch.randn(*s, dtype=torch.bfloat16) * 0.05)(B, Hkv, Sk, Dh)),
        "wq": _w((lambda *s: torch.randn(*s, dtype=torch.bfloat16) * 0.8)(D, Hq * Dh) * (D ** -0.5)),
        "wo": _w((lambda *s: torch.randn(*s, dtype=torch.bfloat16) * 0.05)(Hq * Dh, D) * ((Hq * Dh) ** -0.5)),
    }

def reference(x, k_cache, v_cache, wq, wo):
    B, Sq, D = x.shape
    Hq, Dh = 16, 256
    Hkv = k_cache.shape[1]
    rep = Hq // Hkv
    q = (lambda x, *d: x.permute(*d))((x @ wq).reshape(B, Sq, Hq, Dh), 0, 2, 1, 3)
    k = (lambda x, n, axis: torch.repeat_interleave(x, n, dim=axis))(k_cache, rep, axis=1)
    v = (lambda x, n, axis: torch.repeat_interleave(x, n, dim=axis))(v_cache, rep, axis=1)
    scores = torch.einsum("bhqd,bhkd->bhqk", q, k) * (Dh ** -0.5)
    p = _softmax(scores, axis=-1)
    o = torch.einsum("bhqk,bhkd->bhqd", p, v)
    return (lambda x, *d: x.permute(*d))(o, 0, 2, 1, 3).reshape(B, Sq, Hq * Dh) @ wo
```

```cpp
#include <hip/hip_runtime.h>
#include <hip/hip_bf16.h>
#include <stdint.h>

typedef unsigned u32x4 __attribute__((ext_vector_type(4)));
typedef float f32x4 __attribute__((ext_vector_type(4)));
typedef __bf16 bf16x8 __attribute__((ext_vector_type(8)));
typedef unsigned short u16;

constexpr int D = 448, HQ = 16, HKV = 8, SK = 8192, DH = 256, NSPLIT = 32;

__device__ __forceinline__ float bf2f(unsigned short x) { return __uint_as_float(((unsigned)x) << 16); }
typedef __bf16 bf16x2 __attribute__((ext_vector_type(2)));
typedef float f32x2 __attribute__((ext_vector_type(2)));
__device__ __forceinline__ unsigned cvtpk(float lo, float hi) { f32x2 v = {lo, hi}; return __builtin_bit_cast(unsigned, __builtin_convertvector(v, bf16x2)); }
__device__ __forceinline__ f32x4 mfma16(u32x4 a, u32x4 b, f32x4 c) {
  return __builtin_amdgcn_mfma_f32_16x16x32_bf16(__builtin_bit_cast(bf16x8, a), __builtin_bit_cast(bf16x8, b), c, 0, 0, 0);
}

__global__ void __launch_bounds__(256) k_q(const u16* __restrict__ x, const u16* __restrict__ wq, u16* __restrict__ q) {
  __shared__ float red[128][17];
  const int b = blockIdx.x, t = threadIdx.x;
  const int r = t >> 1, hf = t & 1;
  float acc[8] = {0,0,0,0,0,0,0,0};
  for (int k = r; k < D; k += 128) {
    u32x4 w = *(const u32x4*)(wq + (size_t)k * 4096 + b * 16 + hf * 8);
    float xv = bf2f(x[k]);
#pragma unroll
    for (int j = 0; j < 4; ++j) { acc[2*j] += xv * __uint_as_float(w[j] << 16); acc[2*j+1] += xv * __uint_as_float(w[j] & 0xffff0000u); }
  }
#pragma unroll
  for (int j = 0; j < 8; ++j) red[r][hf * 8 + j] = acc[j];
  __syncthreads();
  if (t < 16) { float s = 0; for (int i = 0; i < 128; ++i) s += red[i][t]; unsigned p = cvtpk(s, s); q[b * 16 + t] = (u16)(p & 0xffff); }
}

__global__ void __launch_bounds__(1024) k_attn(const u16* __restrict__ q, const u16* __restrict__ kc, const u16* __restrict__ vc,
                                               float* __restrict__ po, float* __restrict__ pml) {
  __shared__ __attribute__((aligned(16))) float lds[16 * 512 + 64];
  const int b = blockIdx.x, kvh = b & 7, s = b >> 3;
  const int t = threadIdx.x, lane = t & 63, w = t >> 6, g = lane >> 4, r = lane & 15;
  const size_t kbase = ((size_t)kvh * SK + s * 256 + w * 16);
  u32x4 K[8], V[8];
#pragma unroll
  for (int i = 0; i < 8; ++i) K[i] = __builtin_nontemporal_load((const u32x4*)(kc + (kbase + r) * DH + i * 32 + g * 8));
#pragma unroll
  for (int i = 0; i < 4; ++i)
#pragma unroll
    for (int hf = 0; hf < 2; ++hf) V[i * 2 + hf] = __builtin_nontemporal_load((const u32x4*)(vc + (kbase + 4 * g + i) * DH + hf * 128 + r * 8));
  __builtin_amdgcn_sched_barrier(0);
  const int h = r >> 3, jj = r & 7;
  f32x4 S = {0,0,0,0};
#pragma unroll
  for (int i = 0; i < 8; ++i) {
    u32x4 qf = *(const u32x4*)(q + (kvh * 2 + h) * DH + i * 32 + g * 8);
    S = mfma16(K[i], qf, S);
  }
  float m = fmaxf(fmaxf(S[0], S[1]), fmaxf(S[2], S[3]));
  m = fmaxf(m, __shfl_xor(m, 16)); m = fmaxf(m, __shfl_xor(m, 32));
  float* mx = lds + 16 * 512;
  if (g == 0 && jj == 0) mx[w * 2 + h] = m;
  __syncthreads();
  float M = mx[h];
#pragma unroll
  for (int i = 1; i < 16; ++i) M = fmaxf(M, mx[i * 2 + h]);
  const float C2 = 0.0625f * 1.4426950408889634f;
  float p[4], l = 0;
#pragma unroll
  for (int i = 0; i < 4; ++i) { p[i] = __builtin_amdgcn_exp2f((S[i] - M) * C2); l += p[i]; }
  l += __shfl_xor(l, 16); l += __shfl_xor(l, 32);
  unsigned msk[4];
#pragma unroll
  for (int i = 0; i < 4; ++i) msk[i] = ((jj >> 1) == i) ? ((jj & 1) ? 0xffff0000u : 0x0000ffffu) : 0u;
  f32x4 acc[2] = {{0,0,0,0},{0,0,0,0}};
#pragma unroll
  for (int i = 0; i < 4; ++i) {
    unsigned pp = cvtpk(p[i], p[i]);
    u32x4 A = {pp & msk[0], pp & msk[1], pp & msk[2], pp & msk[3]};
    acc[0] = mfma16(A, V[i * 2], acc[0]);
    acc[1] = mfma16(A, V[i * 2 + 1], acc[1]);
  }
#pragma unroll
  for (int hf = 0; hf < 2; ++hf) *(f32x4*)(lds + w * 512 + (g >> 1) * 256 + hf * 128 + 8 * r + (g & 1) * 4) = acc[hf];
  __syncthreads();
  if (g == 0 && jj == 0) mx[32 + w * 2 + h] = l;
  __syncthreads();
  if (t < 512) {
    float sum = 0;
#pragma unroll
    for (int i = 0; i < 16; ++i) sum += lds[i * 512 + t];
    const int hh = t >> 8, d = t & 255;
    po[((size_t)(kvh * 2 + hh) * NSPLIT + s) * 256 + d] = sum;
  } else if (t < 514) {
    const int hh = t - 512;
    float L = 0, MM = mx[hh];
    for (int i = 0; i < 16; ++i) { L += mx[32 + i * 2 + hh]; MM = fmaxf(MM, mx[i * 2 + hh]); }
    pml[((kvh * 2 + hh) * NSPLIT + s) * 2] = MM;
    pml[((kvh * 2 + hh) * NSPLIT + s) * 2 + 1] = L;
  }
}

__global__ void __launch_bounds__(256) k_comb(const float* __restrict__ po, const float* __restrict__ pml, const u16* __restrict__ wo, float* __restrict__ py) {
  __shared__ float os[16];
  __shared__ float wgt[32];
  const int j = blockIdx.x, t = threadIdx.x, h = j >> 4, d0 = (j & 15) * 16;
  const float C2 = 0.0625f * 1.4426950408889634f;
  if (t < 32) {
    float m = pml[(h * NSPLIT + t) * 2], l = pml[(h * NSPLIT + t) * 2 + 1];
    float M = m;
    for (int o = 1; o < 32; o <<= 1) M = fmaxf(M, __shfl_xor(M, o));
    float wv = __builtin_amdgcn_exp2f((m - M) * C2);
    float L = wv * l;
    for (int o = 1; o < 32; o <<= 1) L += __shfl_xor(L, o);
    wgt[t] = wv / L;
  }
  __syncthreads();
  if (t < 16) {
    float s = 0;
    for (int i = 0; i < 32; ++i) s += wgt[i] * po[((size_t)h * NSPLIT + i) * 256 + d0 + t];
    unsigned p = cvtpk(s, s);
    os[t] = bf2f((u16)(p & 0xffff));
  }
  __syncthreads();
  for (int c = t; c < D; c += 256) {
    float s = 0;
#pragma unroll
    for (int i = 0; i < 16; ++i) s += os[i] * bf2f(wo[(size_t)(h * 256 + d0 + i) * D + c]);
    py[j * D + c] = s;
  }
}
__global__ void __launch_bounds__(64) k_fin(const float* __restrict__ py, u16* __restrict__ out) {
  const int c = blockIdx.x * 64 + threadIdx.x;
  float s = 0;
  for (int i = 0; i < 256; ++i) s += py[i * D + c];
  unsigned p = cvtpk(s, s);
  out[c] = (u16)(p & 0xffff);
}

extern "C" void kernel_launch(void* const* d_in, const int* in_sizes, int n_in,
                              void* d_out, int out_size, void* d_ws, size_t ws_size,
                              hipStream_t stream) {
  const u16* x = (const u16*)d_in[0];
  const u16* kc = (const u16*)d_in[1];
  const u16* vc = (const u16*)d_in[2];
  const u16* wq = (const u16*)d_in[3];
  const u16* wo = (const u16*)d_in[4];
  char* ws = (char*)d_ws;
  u16* q = (u16*)ws;
  float* po = (float*)(ws + 65536);
  float* pml = (float*)(ws + 65536 + 524288);
  float* py = (float*)(ws + 1048576);
  k_q<<<256, 256, 0, stream>>>(x, wq, q);
  k_attn<<<256, 1024, 0, stream>>>(q, kc, vc, po, pml);
  k_comb<<<256, 256, 0, stream>>>(po, pml, wo, py);
  k_fin<<<7, 64, 0, stream>>>(py, (u16*)d_out);
}
```

```cpp
#include <hip/hip_runtime.h>
#include <hip/hip_bf16.h>
#include <stdint.h>

typedef unsigned u32x4 __attribute__((ext_vector_type(4)));
typedef unsigned u32x2 __attribute__((ext_vector_type(2)));
typedef float f32x4 __attribute__((ext_vector_type(4)));
typedef float f32x2 __attribute__((ext_vector_type(2)));
typedef __bf16 bf16x8 __attribute__((ext_vector_type(8)));
typedef __bf16 bf16x2 __attribute__((ext_vector_type(2)));
typedef unsigned short u16;
#define GAS __attribute__((address_space(1)))

#define MAPMODE 0
#define KV_AUX 2
#define WQ_AUX 2
#define WO_AUX 2
#define DBG_TS 0
#define POLL_SLEEP 1
#define WO_POS 1
#define VPRE 0
#define K1SETS 2
#define V1SETS 2

constexpr int DM = 448, SK = 8192, DH = 256;
constexpr int NW = 8, NTHR = NW * 64;
constexpr float C2 = 0.0625f * 1.4426950408889634f;
constexpr unsigned SPIN_CAP = 1u << 18;

constexpr size_t WS_E = 0;
constexpr size_t WS_QG = 4096;
constexpr int QG_STRIDE = 512;
constexpr size_t WS_PG = 262144;
constexpr int PG_STRIDE = 256 * 16;
constexpr size_t WS_YG = WS_PG + 2097152;
constexpr int YG_STRIDE = 224 * 16;
constexpr size_t WS_TS = 8u << 20;

__device__ __forceinline__ unsigned cvtpk(float lo, float hi) { f32x2 v = {lo, hi}; return __builtin_bit_cast(unsigned, __builtin_convertvector(v, bf16x2)); }
__device__ __forceinline__ float bflo(unsigned u) { return __uint_as_float(u << 16); }
__device__ __forceinline__ float bfhi(unsigned u) { return __uint_as_float(u & 0xffff0000u); }
__device__ __forceinline__ f32x4 mfma16(u32x4 a, u32x4 b, f32x4 c) {
  return __builtin_amdgcn_mfma_f32_16x16x32_bf16(__builtin_bit_cast(bf16x8, a), __builtin_bit_cast(bf16x8, b), c, 0, 0, 0);
}
template <int CTRL> __device__ __forceinline__ float dpp(float x) {
  return __builtin_bit_cast(float, __builtin_amdgcn_mov_dpp(__builtin_bit_cast(int, x), CTRL, 0xf, 0xf, true));
}
template <int CTRL> __device__ __forceinline__ unsigned dppu(unsigned x) {
  return (unsigned)__builtin_amdgcn_mov_dpp((int)x, CTRL, 0xf, 0xf, true);
}
constexpr int XOR1 = 0xB1, XOR2 = 0x4E, XOR7 = 0x141, XOR8 = 0x128, ROR4 = 0x124;
__device__ __forceinline__ float x32_sum(float x) {
  auto t = __builtin_amdgcn_permlane32_swap(__float_as_uint(x), __float_as_uint(x), false, false);
  return __uint_as_float(t[0]) + __uint_as_float(t[1]);
}
__device__ __forceinline__ float x16_32_sum(float x) {
  auto s = __builtin_amdgcn_permlane16_swap(__float_as_uint(x), __float_as_uint(x), false, false);
  return x32_sum(__uint_as_float(s[0]) + __uint_as_float(s[1]));
}
__device__ __forceinline__ float x16_32_max(float x) {
  auto s = __builtin_amdgcn_permlane16_swap(__float_as_uint(x), __float_as_uint(x), false, false);
  x = fmaxf(__uint_as_float(s[0]), __uint_as_float(s[1]));
  auto t = __builtin_amdgcn_permlane32_swap(__float_as_uint(x), __float_as_uint(x), false, false);
  return fmaxf(__uint_as_float(t[0]), __uint_as_float(t[1]));
}
__device__ __forceinline__ void wg_barrier() {
  asm volatile("s_waitcnt lgkmcnt(0)\n\ts_barrier" ::: "memory");
}
__device__ __forceinline__ unsigned long long rt() { return __builtin_amdgcn_s_memrealtime(); }
#define STAMP(i) do { } while (0)

__global__ void __launch_bounds__(NTHR, 2)
gqa_decode_fused(const u16* __restrict__ x, const u16* __restrict__ kc, const u16* __restrict__ vc, const u16* __restrict__ wq,
                 const u16* __restrict__ wo, u16* out, char* ws) {
  __shared__ __attribute__((aligned(16))) float lds[4096 + 256 + 512 + 16 + 16 + 16 + 64];
  float* red = lds;
  unsigned* qs = (unsigned*)(lds + 4096);
  float* qpart = lds + 4096 + 256;
  float* mx = qpart + 512;
  float* ls = mx + 16;
  unsigned* odup = (unsigned*)(ls + 16);
  float* fin = ls + 32;
  STAMP(0);
  const int b = blockIdx.x, x8 = b & 7, y = b >> 3;
  const int kvh = MAPMODE ? (y & 7) : x8;
  const int s = MAPMODE ? (x8 * 4 + (y >> 3)) : y;
  const int wg = kvh * 32 + s;
  const int t = threadIdx.x, lane = t & 63, w = __builtin_amdgcn_readfirstlane(t >> 6), g = lane >> 4, r = lane & 15;

  const __amdgpu_buffer_rsrc_t rws = __builtin_amdgcn_make_buffer_rsrc(ws, 0, 16u << 20, 0x00020000);
  const size_t kvoff = ((size_t)kvh * SK + s * 256) * 512;
  const __amdgpu_buffer_rsrc_t rk = __builtin_amdgcn_make_buffer_rsrc((char*)kc + kvoff, 0, 131072, 0x00020000);
  const __amdgpu_buffer_rsrc_t rv = __builtin_amdgcn_make_buffer_rsrc((char*)vc + kvoff, 0, 131072, 0x00020000);
  const __amdgpu_buffer_rsrc_t rwq = __builtin_amdgcn_make_buffer_rsrc((void*)wq, 0, DM * 4096 * 2, 0x00020000);
  const __amdgpu_buffer_rsrc_t rwo = __builtin_amdgcn_make_buffer_rsrc((void*)wo, 0, DM * 4096 * 2, 0x00020000);
  const __amdgpu_buffer_rsrc_t rx = __builtin_amdgcn_make_buffer_rsrc((void*)x, 0, DM * 2, 0x00020000);

  const unsigned Eraw = __hip_atomic_load((GAS unsigned*)(ws + WS_E), __ATOMIC_RELAXED, __HIP_MEMORY_SCOPE_AGENT);

  u32x4 WQ[2]; unsigned short xv[2];
  {
    const int row0 = 112 * (s & 3) + 16 * w + (lane >> 3);
    const int oob = (w < 7) ? 0 : 0x40000000;
#pragma unroll
    for (int i = 0; i < 2; ++i) {
      WQ[i] = __builtin_amdgcn_raw_buffer_load_b128(rwq, (row0 + 8 * i) * 8192 + (lane & 7) * 16 + oob, (kvh * 8 + (s >> 2)) * 128, WQ_AUX);
      xv[i] = __builtin_amdgcn_raw_buffer_load_b16(rx, (row0 + 8 * i) * 2 + oob, 0, 0);
    }
  }
  wg_barrier();
  u32x4 K[16], V[16], WO[4], QG[2];
  const int koff = (r >> 1) * 512 + (r & 1) * 64 + g * 16;
  const int voff = g * 1024 + r * 16;
#pragma unroll
  for (int st = 0; st < K1SETS; ++st)
#pragma unroll
    for (int u = 0; u < 4; ++u) K[st * 4 + u] = __builtin_amdgcn_raw_buffer_load_b128(rk, koff + u * 128, (st * 8 + w) * 4096, KV_AUX);
  __builtin_amdgcn_sched_barrier(0);
  const unsigned T = __builtin_amdgcn_readfirstlane(Eraw) + 1u;
  STAMP(1);
  {
    float a[8];
#pragma unroll
    for (int j = 0; j < 8; ++j) a[j] = 0.f;
#pragma unroll
    for (int i = 0; i < 2; ++i) {
      const float xf = bflo(xv[i]);
#pragma unroll
      for (int j = 0; j < 4; ++j) { a[2 * j] = fmaf(xf, bflo(WQ[i][j]), a[2 * j]); a[2 * j + 1] = fmaf(xf, bfhi(WQ[i][j]), a[2 * j + 1]); }
    }
#pragma unroll
    for (int j = 0; j < 8; ++j) { float v = a[j]; v += dpp<XOR8>(v); a[j] = x16_32_sum(v); }
    if (lane < 8) {
      *(f32x4*)(qpart + w * 64 + lane * 8) = f32x4{a[0], a[1], a[2], a[3]};
      *(f32x4*)(qpart + w * 64 + lane * 8 + 4) = f32x4{a[4], a[5], a[6], a[7]};
    }
  }
  wg_barrier();
  if (w == 0 && lane < 32) {
    f32x2 sum = *(const f32x2*)(qpart + lane * 2);
#pragma unroll
    for (int i = 1; i < 7; ++i) sum += *(const f32x2*)(qpart + i * 64 + lane * 2);
    const u32x4 gr = {__float_as_uint(sum[0]), T, __float_as_uint(sum[1]), T};
    __builtin_amdgcn_raw_buffer_store_b128(gr, rws, (int)WS_QG + wg * QG_STRIDE + lane * 16, 0, 16);
  }
  wg_barrier();
  STAMP(2);
#pragma unroll
  for (int st = K1SETS; st < 4; ++st)
#pragma unroll
    for (int u = 0; u < 4; ++u) K[st * 4 + u] = __builtin_amdgcn_raw_buffer_load_b128(rk, koff + u * 128, (st * 8 + w) * 4096, KV_AUX);
  const int wooff = (w < 4) ? ((4 * g) * (DM * 2) + r * 16 + w * 256) : 0x7ff00000;
#pragma unroll
  for (int st = 0; st < VPRE; ++st)
#pragma unroll
    for (int u = 0; u < 4; ++u) V[st * 4 + u] = __builtin_amdgcn_raw_buffer_load_b128(rv, voff + (u >> 1) * 512 + (u & 1) * 256, (st * 8 + w) * 4096, KV_AUX);
  wg_barrier();
  const int qgoff = (int)WS_QG + (kvh * 32 + w * 4 + (lane >> 5)) * QG_STRIDE + (lane & 31) * 16;
#pragma unroll
  for (int i = 0; i < 2; ++i) QG[i] = __builtin_amdgcn_raw_buffer_load_b128(rws, qgoff + i * 2 * QG_STRIDE, 0, 16);
  wg_barrier();
#pragma unroll
  for (int st = VPRE; st < V1SETS; ++st)
#pragma unroll
    for (int u = 0; u < 4; ++u) V[st * 4 + u] = __builtin_amdgcn_raw_buffer_load_b128(rv, voff + (u >> 1) * 512 + (u & 1) * 256, (st * 8 + w) * 4096, KV_AUX);
  __builtin_amdgcn_sched_barrier(0);
  STAMP(3);
  if (!__all(((QG[0][1] ^ T) | (QG[0][3] ^ T) | (QG[1][1] ^ T) | (QG[1][3] ^ T)) == 0u)) {
#pragma unroll 1
    for (unsigned spins = 0; spins < SPIN_CAP; ++spins) {
      asm volatile("" ::: "memory");
#pragma unroll
      for (int i = 0; i < 2; ++i) QG[i] = __builtin_amdgcn_raw_buffer_load_b128(rws, qgoff + i * 2 * QG_STRIDE, 0, 16);
      if (__all(((QG[0][1] ^ T) | (QG[0][3] ^ T) | (QG[1][1] ^ T) | (QG[1][3] ^ T)) == 0u)) break;
      __builtin_amdgcn_s_sleep(POLL_SLEEP);
    }
  }
  {
    const float q0 = x32_sum(__uint_as_float(QG[0][0]) + __uint_as_float(QG[1][0]));
    const float q1 = x32_sum(__uint_as_float(QG[0][2]) + __uint_as_float(QG[1][2]));
    if (lane < 32) qs[w * 32 + lane] = cvtpk(q0, q1);
  }
  STAMP(4);
  wg_barrier();
  const int h = r >> 3, jj = r & 7;
  u32x4 QB[4];
#pragma unroll
  for (int u = 0; u < 4; ++u) QB[u] = *(const u32x4*)((const char*)qs + h * 512 + u * 128 + (r & 1) * 64 + g * 16);
  float sc[8];
#pragma unroll
  for (int st = 0; st < 4; ++st) {
    f32x4 S = {0.f, 0.f, 0.f, 0.f};
#pragma unroll
    for (int u = 0; u < 4; ++u) S = mfma16(K[st * 4 + u], QB[u], S);
    const float a = (r & 1) ? S[1] : S[0], c = (r & 1) ? S[3] : S[2];
    sc[2 * st] = a + dpp<XOR1>(a);
    sc[2 * st + 1] = c + dpp<XOR1>(c);
  }
  float m = fmaxf(fmaxf(fmaxf(sc[0], sc[1]), fmaxf(sc[2], sc[3])), fmaxf(fmaxf(sc[4], sc[5]), fmaxf(sc[6], sc[7])));
  m = x16_32_max(m);
  if ((lane & 0x37) == 0) mx[w * 2 + h] = m;
  wg_barrier();
  float M;
  {
    const float* p = mx + h;
    M = fmaxf(fmaxf(fmaxf(p[0], p[2]), fmaxf(p[4], p[6])), fmaxf(fmaxf(p[8], p[10]), fmaxf(p[12], p[14])));
  }
  float pv[8], l = 0.f;
#pragma unroll
  for (int i = 0; i < 8; ++i) { pv[i] = __builtin_amdgcn_exp2f((sc[i] - M) * C2); l += pv[i]; }
  l = x16_32_sum(l);
  if ((lane & 0x37) == 0) ls[w * 2 + h] = l;
  unsigned msk[4];
#pragma unroll
  for (int i = 0; i < 4; ++i) msk[i] = ((jj >> 1) == i) ? ((jj & 1) ? 0xffff0000u : 0x0000ffffu) : 0u;
  STAMP(5);
#pragma unroll
  for (int st = V1SETS; st < 4; ++st)
#pragma unroll
    for (int u = 0; u < 4; ++u) V[st * 4 + u] = __builtin_amdgcn_raw_buffer_load_b128(rv, voff + (u >> 1) * 512 + (u & 1) * 256, (st * 8 + w) * 4096, KV_AUX);
  wg_barrier();
#pragma unroll
  for (int st = 0; st < 4; ++st) WO[st] = __builtin_amdgcn_raw_buffer_load_b128(rwo, wooff + st * (DM * 2), wg * 16 * (DM * 2), WO_AUX);
  __builtin_amdgcn_sched_barrier(0);
  wg_barrier();
  float invl, lse;
  {
    const int hp = (t >> 7) & 1;
    float L = 0.f, MM = mx[hp];
#pragma unroll
    for (int i = 0; i < 8; ++i) { L += ls[i * 2 + hp]; MM = fmaxf(MM, mx[i * 2 + hp]); }
    invl = 1.f / L;
    lse = fmaf(MM, C2, __builtin_amdgcn_logf(L));
  }
  f32x4 acc0 = {0.f, 0.f, 0.f, 0.f}, acc1 = {0.f, 0.f, 0.f, 0.f};
#pragma unroll
  for (int st = 0; st < 4; ++st)
#pragma unroll
    for (int i = 0; i < 2; ++i) {
      const unsigned pp = cvtpk(pv[2 * st + i], pv[2 * st + i]);
      const u32x4 A = {pp & msk[0], pp & msk[1], pp & msk[2], pp & msk[3]};
      acc0 = mfma16(A, V[st * 4 + i * 2], acc0);
      acc1 = mfma16(A, V[st * 4 + i * 2 + 1], acc1);
    }
  *(f32x4*)(red + w * 512 + (g >> 1) * 256 + 8 * r + (g & 1) * 4) = acc0;
  *(f32x4*)(red + w * 512 + (g >> 1) * 256 + 128 + 8 * r + (g & 1) * 4) = acc1;
  STAMP(6);
  wg_barrier();
  if (w < 4) {
    f32x2 sum = *(const f32x2*)(red + 2 * t);
#pragma unroll
    for (int i = 1; i < 8; ++i) sum += *(const f32x2*)(red + i * 512 + 2 * t);
    const u32x4 gr = {__float_as_uint(sum[0] * invl), T, __float_as_uint(sum[1] * invl), __float_as_uint(lse)};
    __builtin_amdgcn_raw_buffer_store_b128(gr, rws, (int)WS_PG + wg * PG_STRIDE + t * 16, 0, 16);
  }
  STAMP(7);
  if (w == 4) {
    const int pbase = (int)WS_PG + (kvh * 32 + (lane >> 3)) * PG_STRIDE + (s * 8 + (lane & 7)) * 16;
    u32x4 G[4];
    unsigned spins = 0;
#pragma unroll 1
    while (true) {
#pragma unroll
      for (int i = 0; i < 4; ++i) G[i] = __builtin_amdgcn_raw_buffer_load_b128(rws, pbase, i * 8 * PG_STRIDE, 16);
      unsigned bad = 0;
#pragma unroll
      for (int i = 0; i < 4; ++i) bad |= (G[i][1] ^ T) | (G[i][3] ^ dppu<XOR1>(G[i][3])) | (G[i][3] ^ dppu<XOR2>(G[i][3])) | (G[i][3] ^ dppu<XOR7>(G[i][3]));
      if (__all(bad == 0u) || ++spins > SPIN_CAP) break;
      __builtin_amdgcn_s_sleep(POLL_SLEEP);
      asm volatile("" ::: "memory");
    }
    STAMP(8);
    float Mg = fmaxf(fmaxf(__uint_as_float(G[0][3]), __uint_as_float(G[1][3])), fmaxf(__uint_as_float(G[2][3]), __uint_as_float(G[3][3])));
    Mg = fmaxf(Mg, dpp<XOR8>(Mg)); Mg = x16_32_max(Mg);
    float L = 0.f, o0 = 0.f, o1 = 0.f;
#pragma unroll
    for (int i = 0; i < 4; ++i) {
      const float wt = __builtin_amdgcn_exp2f(__uint_as_float(G[i][3]) - Mg);
      L += wt; o0 = fmaf(wt, __uint_as_float(G[i][0]), o0); o1 = fmaf(wt, __uint_as_float(G[i][2]), o1);
    }
    L += dpp<XOR8>(L); o0 += dpp<XOR8>(o0); o1 += dpp<XOR8>(o1);
    L = x16_32_sum(L); o0 = x16_32_sum(o0); o1 = x16_32_sum(o1);
    const float inv = 1.f / L;
    o0 *= inv; o1 *= inv;
    if (lane < 8) *(u32x2*)(odup + 2 * lane) = u32x2{cvtpk(o0, o0), cvtpk(o1, o1)};
  }
  wg_barrier();
  if (w < 4) {
    const u32x4 ov = *(const u32x4*)(odup + 4 * g);
    f32x4 ya = {0.f, 0.f, 0.f, 0.f};
#pragma unroll
    for (int st = 0; st < 4; ++st) {
      const u32x4 A = {ov[st] & msk[0], ov[st] & msk[1], ov[st] & msk[2], ov[st] & msk[3]};
      ya = mfma16(A, WO[st], ya);
    }
    const float y0 = (g >> 1) ? ya[2] : ya[0], y1 = (g >> 1) ? ya[3] : ya[1];
    const int gi = w * 64 + 4 * r + (g & 1) * 2 + (g >> 1);
    if (gi < 224) {
      const u32x4 gr = {__float_as_uint(y0), T, __float_as_uint(y1), T};
      __builtin_amdgcn_raw_buffer_store_b128(gr, rws, (int)WS_YG + wg * YG_STRIDE + gi * 16, 0, 16);
    }
  }
  STAMP(9);
  if (y < 7) {
    const int c = x8 * 7 + y;
    const int ybase = (int)WS_YG + (32 * w + (lane >> 2)) * YG_STRIDE + (c * 4 + (lane & 3)) * 16;
    u32x4 G[2];
    unsigned spins = 0;
#pragma unroll 1
    while (true) {
#pragma unroll
      for (int i = 0; i < 2; ++i) G[i] = __builtin_amdgcn_raw_buffer_load_b128(rws, ybase, i * 16 * YG_STRIDE, 16);
      const unsigned bad = (G[0][1] ^ T) | (G[0][3] ^ T) | (G[1][1] ^ T) | (G[1][3] ^ T);
      if (__all(bad == 0u) || ++spins > SPIN_CAP) break;
      __builtin_amdgcn_s_sleep(POLL_SLEEP);
      asm volatile("" ::: "memory");
    }
    STAMP(10);
    float y0 = __uint_as_float(G[0][0]) + __uint_as_float(G[1][0]);
    float y1 = __uint_as_float(G[0][2]) + __uint_as_float(G[1][2]);
    y0 += dpp<ROR4>(y0); y1 += dpp<ROR4>(y1);
    y0 += dpp<XOR8>(y0); y1 += dpp<XOR8>(y1);
    y0 = x16_32_sum(y0); y1 = x16_32_sum(y1);
    if (lane < 4) *(f32x2*)(fin + w * 8 + lane * 2) = f32x2{y0, y1};
    wg_barrier();
    if (t < 4) {
      f32x2 sum = *(const f32x2*)(fin + t * 2);
#pragma unroll
      for (int i = 1; i < 8; ++i) sum += *(const f32x2*)(fin + i * 8 + t * 2);
      ((unsigned*)out)[c * 4 + t] = cvtpk(sum[0], sum[1]);
      if (b == 0 && t == 0) __hip_atomic_store((GAS unsigned*)(ws + WS_E), T, __ATOMIC_RELAXED, __HIP_MEMORY_SCOPE_AGENT);
    }
  }
}

extern "C" void kernel_launch(void* const* d_in, const int* in_sizes, int n_in,
                              void* d_out, int out_size, void* d_ws, size_t ws_size,
                              hipStream_t stream) {
  gqa_decode_fused<<<256, NTHR, 0, stream>>>((const u16*)d_in[0], (const u16*)d_in[1], (const u16*)d_in[2], (const u16*)d_in[3],
                                             (const u16*)d_in[4], (u16*)d_out, (char*)d_ws);
}
```

```cpp
#include <hip/hip_runtime.h>
#include <hip/hip_bf16.h>
#include <stdint.h>

typedef unsigned u32x4 __attribute__((ext_vector_type(4)));
typedef unsigned u32x2 __attribute__((ext_vector_type(2)));
typedef float f32x4 __attribute__((ext_vector_type(4)));
typedef float f32x2 __attribute__((ext_vector_type(2)));
typedef __bf16 bf16x8 __attribute__((ext_vector_type(8)));
typedef __bf16 bf16x2 __attribute__((ext_vector_type(2)));
typedef unsigned short u16;
#define GAS __attribute__((address_space(1)))

#define MAPMODE 0
#define KV_AUX 2
#define WQ_AUX 2
#define WO_AUX 2
#define DBG_TS 0
#define POLL_SLEEP 1
#define WO_POS 2
#define VPRE 0
#define ABL_NOTAIL 0
#define TA 80
#define TB 40
#define TGATE 0
#define PK2 63
#define PV1 63
#define PV2 63
#define EVENONLY 0
#define PACEW(n) do { if ((n) < 63 && (!EVENONLY || paced)) asm volatile("s_waitcnt vmcnt(%0)" :: "i"((n) < 63 ? (n) : 0) : "memory"); } while (0)
#define K1SETS 2
#define V1SETS 2

constexpr int DM = 448, SK = 8192, DH = 256;
constexpr int NW = 8, NTHR = NW * 64;
constexpr float C2 = 0.0625f * 1.4426950408889634f;
constexpr unsigned SPIN_CAP = 1u << 18;

constexpr size_t WS_E = 0;
constexpr size_t WS_QG = 4096;
constexpr int QG_STRIDE = 512;
constexpr size_t WS_PG = 262144;
constexpr int PG_STRIDE = 256 * 16;
constexpr size_t WS_YG = WS_PG + 2097152;
constexpr int YG_STRIDE = 224 * 16;
constexpr size_t WS_TS = 8u << 20;

__device__ __forceinline__ unsigned cvtpk(float lo, float hi) { f32x2 v = {lo, hi}; return __builtin_bit_cast(unsigned, __builtin_convertvector(v, bf16x2)); }
__device__ __forceinline__ float bflo(unsigned u) { return __uint_as_float(u << 16); }
__device__ __forceinline__ float bfhi(unsigned u) { return __uint_as_float(u & 0xffff0000u); }
__device__ __forceinline__ f32x4 mfma16(u32x4 a, u32x4 b, f32x4 c) {
  return __builtin_amdgcn_mfma_f32_16x16x32_bf16(__builtin_bit_cast(bf16x8, a), __builtin_bit_cast(bf16x8, b), c, 0, 0, 0);
}
template <int CTRL> __device__ __forceinline__ float dpp(float x) {
  return __builtin_bit_cast(float, __builtin_amdgcn_mov_dpp(__builtin_bit_cast(int, x), CTRL, 0xf, 0xf, true));
}
template <int CTRL> __device__ __forceinline__ unsigned dppu(unsigned x) {
  return (unsigned)__builtin_amdgcn_mov_dpp((int)x, CTRL, 0xf, 0xf, true);
}
constexpr int XOR1 = 0xB1, XOR2 = 0x4E, XOR7 = 0x141, XOR8 = 0x128, ROR4 = 0x124;
__device__ __forceinline__ float x32_sum(float x) {
  auto t = __builtin_amdgcn_permlane32_swap(__float_as_uint(x), __float_as_uint(x), false, false);
  return __uint_as_float(t[0]) + __uint_as_float(t[1]);
}
__device__ __forceinline__ float x16_32_sum(float x) {
  auto s = __builtin_amdgcn_permlane16_swap(__float_as_uint(x), __float_as_uint(x), false, false);
  return x32_sum(__uint_as_float(s[0]) + __uint_as_float(s[1]));
}
__device__ __forceinline__ float x16_32_max(float x) {
  auto s = __builtin_amdgcn_permlane16_swap(__float_as_uint(x), __float_as_uint(x), false, false);
  x = fmaxf(__uint_as_float(s[0]), __uint_as_float(s[1]));
  auto t = __builtin_amdgcn_permlane32_swap(__float_as_uint(x), __float_as_uint(x), false, false);
  return fmaxf(__uint_as_float(t[0]), __uint_as_float(t[1]));
}
__device__ __forceinline__ void wg_barrier() {
  asm volatile("s_waitcnt lgkmcnt(0)\n\ts_barrier" ::: "memory");
}
__device__ __forceinline__ unsigned long long rt() { return __builtin_amdgcn_s_memrealtime(); }
#define STAMP(i) do { } while (0)

__global__ void __launch_bounds__(NTHR, 2)
gqa_decode_fused(const u16* __restrict__ x, const u16* __restrict__ kc, const u16* __restrict__ vc, const u16* __restrict__ wq,
                 const u16* __restrict__ wo, u16* out, char* ws) {
  __shared__ __attribute__((aligned(16))) float lds[4096 + 256 + 512 + 16 + 16 + 16 + 64];
  float* red = lds;
  unsigned* qs = (unsigned*)(lds + 4096);
  float* qpart = lds + 4096 + 256;
  float* mx = qpart + 512;
  float* ls = mx + 16;
  unsigned* odup = (unsigned*)(ls + 16);
  float* fin = ls + 32;
  const unsigned t_start = (unsigned)rt();
  STAMP(0);
  const int b = blockIdx.x, x8 = b & 7, y = b >> 3;
  const int kvh = MAPMODE ? (y & 7) : x8;
  const int s = MAPMODE ? (x8 * 4 + (y >> 3)) : y;
  const int wg = kvh * 32 + s;
  const bool paced = EVENONLY ? (((unsigned)__builtin_amdgcn_s_getreg(20 | (3 << 11)) & 1u) == 0u) : true;
  const int t = threadIdx.x, lane = t & 63, w = __builtin_amdgcn_readfirstlane(t >> 6), g = lane >> 4, r = lane & 15;

  const __amdgpu_buffer_rsrc_t rws = __builtin_amdgcn_make_buffer_rsrc(ws, 0, 16u << 20, 0x00020000);
  const size_t kvoff = ((size_t)kvh * SK + s * 256) * 512;
  const __amdgpu_buffer_rsrc_t rk = __builtin_amdgcn_make_buffer_rsrc((char*)kc + kvoff, 0, 131072, 0x00020000);
  const __amdgpu_buffer_rsrc_t rv = __builtin_amdgcn_make_buffer_rsrc((char*)vc + kvoff, 0, 131072, 0x00020000);
  const __amdgpu_buffer_rsrc_t rwq = __builtin_amdgcn_make_buffer_rsrc((void*)wq, 0, DM * 4096 * 2, 0x00020000);
  const __amdgpu_buffer_rsrc_t rwo = __builtin_amdgcn_make_buffer_rsrc((void*)wo, 0, DM * 4096 * 2, 0x00020000);
  const __amdgpu_buffer_rsrc_t rx = __builtin_amdgcn_make_buffer_rsrc((void*)x, 0, DM * 2, 0x00020000);

  const unsigned Eraw = __hip_atomic_load((GAS unsigned*)(ws + WS_E), __ATOMIC_RELAXED, __HIP_MEMORY_SCOPE_AGENT);

  u32x4 WQ[2]; unsigned short xv[2];
  {
    const int row0 = 112 * (s & 3) + 16 * w + (lane >> 3);
    const int oob = (w < 7) ? 0 : 0x40000000;
#pragma unroll
    for (int i = 0; i < 2; ++i) {
      WQ[i] = __builtin_amdgcn_raw_buffer_load_b128(rwq, (row0 + 8 * i) * 8192 + (lane & 7) * 16 + oob, (kvh * 8 + (s >> 2)) * 128, WQ_AUX);
      xv[i] = __builtin_amdgcn_raw_buffer_load_b16(rx, (row0 + 8 * i) * 2 + oob, 0, 0);
    }
  }
  wg_barrier();
  u32x4 K[16], V[16], WO[4], QG[2];
  const int koff = (r >> 1) * 512 + (r & 1) * 64 + g * 16;
  const int voff = g * 1024 + r * 16;
#pragma unroll
  for (int st = 0; st < K1SETS; ++st)
#pragma unroll
    for (int u = 0; u < 4; ++u) K[st * 4 + u] = __builtin_amdgcn_raw_buffer_load_b128(rk, koff + u * 128, (st * 8 + w) * 4096, KV_AUX);
  __builtin_amdgcn_sched_barrier(0);
  const unsigned T = __builtin_amdgcn_readfirstlane(Eraw) + 1u;
  STAMP(1);
  {
    float a[8];
#pragma unroll
    for (int j = 0; j < 8; ++j) a[j] = 0.f;
#pragma unroll
    for (int i = 0; i < 2; ++i) {
      const float xf = bflo(xv[i]);
#pragma unroll
      for (int j = 0; j < 4; ++j) { a[2 * j] = fmaf(xf, bflo(WQ[i][j]), a[2 * j]); a[2 * j + 1] = fmaf(xf, bfhi(WQ[i][j]), a[2 * j + 1]); }
    }
#pragma unroll
    for (int j = 0; j < 8; ++j) { float v = a[j]; v += dpp<XOR8>(v); a[j] = x16_32_sum(v); }
    if (lane < 8) {
      *(f32x4*)(qpart + w * 64 + lane * 8) = f32x4{a[0], a[1], a[2], a[3]};
      *(f32x4*)(qpart + w * 64 + lane * 8 + 4) = f32x4{a[4], a[5], a[6], a[7]};
    }
  }
  wg_barrier();
  if (w == 0 && lane < 32) {
    f32x2 sum = *(const f32x2*)(qpart + lane * 2);
#pragma unroll
    for (int i = 1; i < 7; ++i) sum += *(const f32x2*)(qpart + i * 64 + lane * 2);
    const u32x4 gr = {__float_as_uint(sum[0]), T, __float_as_uint(sum[1]), T};
    __builtin_amdgcn_raw_buffer_store_b128(gr, rws, (int)WS_QG + wg * QG_STRIDE + lane * 16, 0, 16);
  }
  wg_barrier();
  STAMP(2);
#pragma unroll
  for (int st = K1SETS; st < 4; ++st)
#pragma unroll
    for (int u = 0; u < 4; ++u) { PACEW(PK2); K[st * 4 + u] = __builtin_amdgcn_raw_buffer_load_b128(rk, koff + u * 128, (st * 8 + w) * 4096, KV_AUX); }
  const int wooff = (w < 4) ? ((4 * g) * (DM * 2) + r * 16 + w * 256) : 0x7ff00000;
#pragma unroll
  for (int st = 0; st < VPRE; ++st)
#pragma unroll
    for (int u = 0; u < 4; ++u) V[st * 4 + u] = __builtin_amdgcn_raw_buffer_load_b128(rv, voff + (u >> 1) * 512 + (u & 1) * 256, (st * 8 + w) * 4096, KV_AUX);
  wg_barrier();
  const int qgoff = (int)WS_QG + (kvh * 32 + w * 4 + (lane >> 5)) * QG_STRIDE + (lane & 31) * 16;
#pragma unroll
  for (int i = 0; i < 2; ++i) QG[i] = __builtin_amdgcn_raw_buffer_load_b128(rws, qgoff + i * 2 * QG_STRIDE, 0, 16);
  wg_barrier();
#pragma unroll
  for (int st = VPRE; st < V1SETS; ++st)
#pragma unroll
    for (int u = 0; u < 4; ++u) { PACEW(PV1); V[st * 4 + u] = __builtin_amdgcn_raw_buffer_load_b128(rv, voff + (u >> 1) * 512 + (u & 1) * 256, (st * 8 + w) * 4096, KV_AUX); }
  __builtin_amdgcn_sched_barrier(0);
  STAMP(3);
  if (!__all(((QG[0][1] ^ T) | (QG[0][3] ^ T) | (QG[1][1] ^ T) | (QG[1][3] ^ T)) == 0u)) {
#pragma unroll 1
    for (unsigned spins = 0; spins < SPIN_CAP; ++spins) {
      asm volatile("" ::: "memory");
#pragma unroll
      for (int i = 0; i < 2; ++i) QG[i] = __builtin_amdgcn_raw_buffer_load_b128(rws, qgoff + i * 2 * QG_STRIDE, 0, 16);
      if (__all(((QG[0][1] ^ T) | (QG[0][3] ^ T) | (QG[1][1] ^ T) | (QG[1][3] ^ T)) == 0u)) break;
      __builtin_amdgcn_s_sleep(POLL_SLEEP);
    }
  }
  {
    const float q0 = x32_sum(__uint_as_float(QG[0][0]) + __uint_as_float(QG[1][0]));
    const float q1 = x32_sum(__uint_as_float(QG[0][2]) + __uint_as_float(QG[1][2]));
    if (lane < 32) qs[w * 32 + lane] = cvtpk(q0, q1);
  }
  STAMP(4);
  wg_barrier();
  const int h = r >> 3, jj = r & 7;
  u32x4 QB[4];
#pragma unroll
  for (int u = 0; u < 4; ++u) QB[u] = *(const u32x4*)((const char*)qs + h * 512 + u * 128 + (r & 1) * 64 + g * 16);
  float sc[8];
#pragma unroll
  for (int st = 0; st < 4; ++st) {
    f32x4 S = {0.f, 0.f, 0.f, 0.f};
#pragma unroll
    for (int u = 0; u < 4; ++u) S = mfma16(K[st * 4 + u], QB[u], S);
    const float a = (r & 1) ? S[1] : S[0], c = (r & 1) ? S[3] : S[2];
    sc[2 * st] = a + dpp<XOR1>(a);
    sc[2 * st + 1] = c + dpp<XOR1>(c);
  }
  float m = fmaxf(fmaxf(fmaxf(sc[0], sc[1]), fmaxf(sc[2], sc[3])), fmaxf(fmaxf(sc[4], sc[5]), fmaxf(sc[6], sc[7])));
  m = x16_32_max(m);
  if ((lane & 0x37) == 0) mx[w * 2 + h] = m;
  wg_barrier();
  float M;
  {
    const float* p = mx + h;
    M = fmaxf(fmaxf(fmaxf(p[0], p[2]), fmaxf(p[4], p[6])), fmaxf(fmaxf(p[8], p[10]), fmaxf(p[12], p[14])));
  }
  float pv[8], l = 0.f;
#pragma unroll
  for (int i = 0; i < 8; ++i) { pv[i] = __builtin_amdgcn_exp2f((sc[i] - M) * C2); l += pv[i]; }
  l = x16_32_sum(l);
  if ((lane & 0x37) == 0) ls[w * 2 + h] = l;
  unsigned msk[4];
#pragma unroll
  for (int i = 0; i < 4; ++i) msk[i] = ((jj >> 1) == i) ? ((jj & 1) ? 0xffff0000u : 0x0000ffffu) : 0u;
  STAMP(5);
#pragma unroll
  for (int st = V1SETS; st < 4; ++st)
#pragma unroll
    for (int u = 0; u < 4; ++u) { PACEW(PV2); V[st * 4 + u] = __builtin_amdgcn_raw_buffer_load_b128(rv, voff + (u >> 1) * 512 + (u & 1) * 256, (st * 8 + w) * 4096, KV_AUX); }
  __builtin_amdgcn_sched_barrier(0);
  wg_barrier();
  float invl, lse;
  {
    const int hp = (t >> 7) & 1;
    float L = 0.f, MM = mx[hp];
#pragma unroll
    for (int i = 0; i < 8; ++i) { L += ls[i * 2 + hp]; MM = fmaxf(MM, mx[i * 2 + hp]); }
    invl = 1.f / L;
    lse = fmaf(MM, C2, __builtin_amdgcn_logf(L));
  }
  f32x4 acc0 = {0.f, 0.f, 0.f, 0.f}, acc1 = {0.f, 0.f, 0.f, 0.f};
#pragma unroll
  for (int st = 0; st < 4; ++st)
#pragma unroll
    for (int i = 0; i < 2; ++i) {
      const unsigned pp = cvtpk(pv[2 * st + i], pv[2 * st + i]);
      const u32x4 A = {pp & msk[0], pp & msk[1], pp & msk[2], pp & msk[3]};
      acc0 = mfma16(A, V[st * 4 + i * 2], acc0);
      acc1 = mfma16(A, V[st * 4 + i * 2 + 1], acc1);
    }
  *(f32x4*)(red + w * 512 + (g >> 1) * 256 + 8 * r + (g & 1) * 4) = acc0;
  *(f32x4*)(red + w * 512 + (g >> 1) * 256 + 128 + 8 * r + (g & 1) * 4) = acc1;
  STAMP(6);
  wg_barrier();
  if (ABL_NOTAIL) { if (red[t] == 12345.678f) out[0] = 1; goto done; }
  {
  if (w < 4) {
    f32x2 pr[8];
#pragma unroll
    for (int i = 0; i < 8; ++i) pr[i] = *(const f32x2*)(red + i * 512 + 2 * t);
    const f32x2 sum = ((pr[0] + pr[1]) + (pr[2] + pr[3])) + ((pr[4] + pr[5]) + (pr[6] + pr[7]));
    const u32x4 gr = {__float_as_uint(sum[0] * invl), T, __float_as_uint(sum[1] * invl), __float_as_uint(lse)};
    __builtin_amdgcn_raw_buffer_store_b128(gr, rws, (int)WS_PG + wg * PG_STRIDE + t * 16, 0, 16);
    while ((int)((unsigned)rt() - t_start) < TGATE) __builtin_amdgcn_s_sleep(2);
#pragma unroll
    for (int st = 0; st < 4; ++st) WO[st] = __builtin_amdgcn_raw_buffer_load_b128(rwo, wooff + st * (DM * 2), wg * 16 * (DM * 2), WO_AUX);
  }
  STAMP(7);
  if (w == 4) {
    const int pbase = (int)WS_PG + (kvh * 32 + (lane >> 3)) * PG_STRIDE + (s * 8 + (lane & 7)) * 16;
    u32x4 G[4];
    unsigned spins = 0;
    if (TA > 0) { const unsigned o = (unsigned)rt(); while ((int)((unsigned)rt() - o) < TA) __builtin_amdgcn_s_sleep(1); }
#pragma unroll 1
    while (true) {
#pragma unroll
      for (int i = 0; i < 4; ++i) G[i] = __builtin_amdgcn_raw_buffer_load_b128(rws, pbase, i * 8 * PG_STRIDE, 16);
      unsigned bad = 0;
#pragma unroll
      for (int i = 0; i < 4; ++i) bad |= (G[i][1] ^ T) | (G[i][3] ^ dppu<XOR1>(G[i][3])) | (G[i][3] ^ dppu<XOR2>(G[i][3])) | (G[i][3] ^ dppu<XOR7>(G[i][3]));
      if (__all(bad == 0u) || ++spins > SPIN_CAP) break;
      __builtin_amdgcn_s_sleep(POLL_SLEEP);
      asm volatile("" ::: "memory");
    }
    STAMP(8);
    float Mg = fmaxf(fmaxf(__uint_as_float(G[0][3]), __uint_as_float(G[1][3])), fmaxf(__uint_as_float(G[2][3]), __uint_as_float(G[3][3])));
    Mg = fmaxf(Mg, dpp<XOR8>(Mg)); Mg = x16_32_max(Mg);
    float L = 0.f, o0 = 0.f, o1 = 0.f;
#pragma unroll
    for (int i = 0; i < 4; ++i) {
      const float wt = __builtin_amdgcn_exp2f(__uint_as_float(G[i][3]) - Mg);
      L += wt; o0 = fmaf(wt, __uint_as_float(G[i][0]), o0); o1 = fmaf(wt, __uint_as_float(G[i][2]), o1);
    }
    L += dpp<XOR8>(L); o0 += dpp<XOR8>(o0); o1 += dpp<XOR8>(o1);
    L = x16_32_sum(L); o0 = x16_32_sum(o0); o1 = x16_32_sum(o1);
    const float inv = __builtin_amdgcn_rcpf(L);
    o0 *= inv; o1 *= inv;
    if (lane < 8) *(u32x2*)(odup + 2 * lane) = u32x2{cvtpk(o0, o0), cvtpk(o1, o1)};
  }
  wg_barrier();
  if (w < 4) {
    const u32x4 ov = *(const u32x4*)(odup + 4 * g);
    f32x4 yp[4];
#pragma unroll
    for (int st = 0; st < 4; ++st) {
      const u32x4 A = {ov[st] & msk[0], ov[st] & msk[1], ov[st] & msk[2], ov[st] & msk[3]};
      yp[st] = mfma16(A, WO[st], f32x4{0.f, 0.f, 0.f, 0.f});
    }
    const f32x4 ya = (yp[0] + yp[1]) + (yp[2] + yp[3]);
    const float y0 = (g >> 1) ? ya[2] : ya[0], y1 = (g >> 1) ? ya[3] : ya[1];
    const int gi = w * 64 + 4 * r + (g & 1) * 2 + (g >> 1);
    if (gi < 224) {
      const u32x4 gr = {__float_as_uint(y0), T, __float_as_uint(y1), T};
      __builtin_amdgcn_raw_buffer_store_b128(gr, rws, (int)WS_YG + wg * YG_STRIDE + gi * 16, 0, 16);
    }
  }
  STAMP(9);
  if (y < 7) {
    const int c = x8 * 7 + y;
    const int ybase = (int)WS_YG + (32 * w + (lane >> 2)) * YG_STRIDE + (c * 4 + (lane & 3)) * 16;
    u32x4 G[2];
    unsigned spins = 0;
    if (TB > 0) { const unsigned o = (unsigned)rt(); while ((int)((unsigned)rt() - o) < TB) __builtin_amdgcn_s_sleep(1); }
#pragma unroll 1
    while (true) {
#pragma unroll
      for (int i = 0; i < 2; ++i) G[i] = __builtin_amdgcn_raw_buffer_load_b128(rws, ybase, i * 16 * YG_STRIDE, 16);
      const unsigned bad = (G[0][1] ^ T) | (G[0][3] ^ T) | (G[1][1] ^ T) | (G[1][3] ^ T);
      if (__all(bad == 0u) || ++spins > SPIN_CAP) break;
      __builtin_amdgcn_s_sleep(POLL_SLEEP);
      asm volatile("" ::: "memory");
    }
    STAMP(10);
    float y0 = __uint_as_float(G[0][0]) + __uint_as_float(G[1][0]);
    float y1 = __uint_as_float(G[0][2]) + __uint_as_float(G[1][2]);
    y0 += dpp<ROR4>(y0); y1 += dpp<ROR4>(y1);
    y0 += dpp<XOR8>(y0); y1 += dpp<XOR8>(y1);
    y0 = x16_32_sum(y0); y1 = x16_32_sum(y1);
    if (lane < 4) *(f32x2*)(fin + w * 8 + lane * 2) = f32x2{y0, y1};
    wg_barrier();
    if (t < 4) {
      f32x2 pr[8];
#pragma unroll
      for (int i = 0; i < 8; ++i) pr[i] = *(const f32x2*)(fin + i * 8 + t * 2);
      const f32x2 sum = ((pr[0] + pr[1]) + (pr[2] + pr[3])) + ((pr[4] + pr[5]) + (pr[6] + pr[7]));
      __hip_atomic_store((GAS unsigned*)out + c * 4 + t, cvtpk(sum[0], sum[1]), __ATOMIC_RELAXED, __HIP_MEMORY_SCOPE_AGENT);
      if (b == 0 && t == 0) __hip_atomic_store((GAS unsigned*)(ws + WS_E), T, __ATOMIC_RELAXED, __HIP_MEMORY_SCOPE_AGENT);
    }
  }
  }
done:
  ;
}

extern "C" void kernel_launch(void* const* d_in, const int* in_sizes, int n_in,
                              void* d_out, int out_size, void* d_ws, size_t ws_size,
                              hipStream_t stream) {
  gqa_decode_fused<<<256, NTHR, 0, stream>>>((const u16*)d_in[0], (const u16*)d_in[1], (const u16*)d_in[2], (const u16*)d_in[3],
                                             (const u16*)d_in[4], (u16*)d_out, (char*)d_ws);
}
```

```cpp
#include <hip/hip_runtime.h>
#include <hip/hip_bf16.h>
#include <stdint.h>

typedef unsigned u32x4 __attribute__((ext_vector_type(4)));
typedef unsigned u32x2 __attribute__((ext_vector_type(2)));
typedef float f32x4 __attribute__((ext_vector_type(4)));
typedef float f32x2 __attribute__((ext_vector_type(2)));
typedef __bf16 bf16x8 __attribute__((ext_vector_type(8)));
typedef __bf16 bf16x2 __attribute__((ext_vector_type(2)));
typedef unsigned short u16;
#define GAS __attribute__((address_space(1)))

#define MAPMODE 0
#define KV_AUX 2
#define WQ_AUX 2
#define WO_AUX 2
#define DBG_TS 0
#define POLL_SLEEP 1
#define WO_POS 2
#define VPRE 1
#define ABL_NOTAIL 0
#define TA 80
#define TB 40
#define TGATE 0
#define PK2 63
#define PV1 63
#define PV2 63
#define EVENONLY 0
#define PACEW(n) do { if ((n) < 63 && (!EVENONLY || paced)) asm volatile("s_waitcnt vmcnt(%0)" :: "i"((n) < 63 ? (n) : 0) : "memory"); } while (0)
#define MSET 0
#define SETX(st) (MSET == 0 ? s * 32 + (st) * 8 + w : MSET == 1 ? (st) * 256 + s * 8 + w : MSET == 2 ? s * 32 + w * 4 + (st) : (st) * 256 + w * 32 + s)
#define TAABS 0
#define TBABS 0
#define TLATE 790
#define STRIP 0
#define NRED 28
#define K1SETS 2
#define V1SETS 2

constexpr int DM = 448, SK = 8192, DH = 256;
constexpr int NW = 8, NTHR = NW * 64;
constexpr float C2 = 0.0625f * 1.4426950408889634f;
constexpr unsigned SPIN_CAP = 1u << 18;

constexpr size_t WS_E = 0;
constexpr size_t WS_QG = 4096;
constexpr int QG_STRIDE = 512;
constexpr size_t WS_PG = 262144;
constexpr int PG_STRIDE = 256 * 16;
constexpr size_t WS_YG = WS_PG + 2097152;
constexpr int YG_STRIDE = 224 * 16;
constexpr size_t WS_TS = 8u << 20;

__device__ __forceinline__ unsigned cvtpk(float lo, float hi) { f32x2 v = {lo, hi}; return __builtin_bit_cast(unsigned, __builtin_convertvector(v, bf16x2)); }
__device__ __forceinline__ float bflo(unsigned u) { return __uint_as_float(u << 16); }
__device__ __forceinline__ float bfhi(unsigned u) { return __uint_as_float(u & 0xffff0000u); }
__device__ __forceinline__ f32x4 mfma16(u32x4 a, u32x4 b, f32x4 c) {
  return __builtin_amdgcn_mfma_f32_16x16x32_bf16(__builtin_bit_cast(bf16x8, a), __builtin_bit_cast(bf16x8, b), c, 0, 0, 0);
}
template <int CTRL> __device__ __forceinline__ float dpp(float x) {
  return __builtin_bit_cast(float, __builtin_amdgcn_mov_dpp(__builtin_bit_cast(int, x), CTRL, 0xf, 0xf, true));
}
template <int CTRL> __device__ __forceinline__ unsigned dppu(unsigned x) {
  return (unsigned)__builtin_amdgcn_mov_dpp((int)x, CTRL, 0xf, 0xf, true);
}
constexpr int XOR1 = 0xB1, XOR2 = 0x4E, XOR7 = 0x141, XOR8 = 0x128, ROR4 = 0x124;
__device__ __forceinline__ float x32_sum(float x) {
  auto t = __builtin_amdgcn_permlane32_swap(__float_as_uint(x), __float_as_uint(x), false, false);
  return __uint_as_float(t[0]) + __uint_as_float(t[1]);
}
__device__ __forceinline__ float x16_32_sum(float x) {
  auto s = __builtin_amdgcn_permlane16_swap(__float_as_uint(x), __float_as_uint(x), false, false);
  return x32_sum(__uint_as_float(s[0]) + __uint_as_float(s[1]));
}
__device__ __forceinline__ float x16_32_max(float x) {
  auto s = __builtin_amdgcn_permlane16_swap(__float_as_uint(x), __float_as_uint(x), false, false);
  x = fmaxf(__uint_as_float(s[0]), __uint_as_float(s[1]));
  auto t = __builtin_amdgcn_permlane32_swap(__float_as_uint(x), __float_as_uint(x), false, false);
  return fmaxf(__uint_as_float(t[0]), __uint_as_float(t[1]));
}
__device__ __forceinline__ void wg_barrier() {
  asm volatile("s_waitcnt lgkmcnt(0)\n\ts_barrier" ::: "memory");
}
__device__ __forceinline__ unsigned long long rt() { return __builtin_amdgcn_s_memrealtime(); }
#define STAMP(i) do { } while (0)

__global__ void __launch_bounds__(NTHR, 2)
gqa_decode_fused(const u16* __restrict__ x, const u16* __restrict__ kc, const u16* __restrict__ vc, const u16* __restrict__ wq,
                 const u16* __restrict__ wo, u16* out, char* ws) {
  __shared__ __attribute__((aligned(16))) float lds[4096 + 256 + 512 + 16 + 16 + 16 + 128];
  float* red = lds;
  unsigned* qs = (unsigned*)(lds + 4096);
  float* qpart = lds + 4096 + 256;
  float* mx = qpart + 512;
  float* ls = mx + 16;
  unsigned* odup = (unsigned*)(ls + 16);
  float* fin = ls + 32;
  const unsigned t_start = (unsigned)rt();
  STAMP(0);
  const int b = blockIdx.x, x8 = b & 7, y = b >> 3;
  const int kvh = MAPMODE ? (y & 7) : x8;
  const int s = MAPMODE ? (x8 * 4 + (y >> 3)) : y;
  const int wg = kvh * 32 + s;
  const bool paced = EVENONLY ? (((unsigned)__builtin_amdgcn_s_getreg(20 | (3 << 11)) & 1u) == 0u) : true;
  const int t = threadIdx.x, lane = t & 63, w = __builtin_amdgcn_readfirstlane(t >> 6), g = lane >> 4, r = lane & 15;

  const __amdgpu_buffer_rsrc_t rws = __builtin_amdgcn_make_buffer_rsrc(ws, 0, 16u << 20, 0x00020000);
  const size_t kvoff = (size_t)kvh * SK * 512;
  const __amdgpu_buffer_rsrc_t rk = __builtin_amdgcn_make_buffer_rsrc((char*)kc + kvoff, 0, SK * 512, 0x00020000);
  const __amdgpu_buffer_rsrc_t rv = __builtin_amdgcn_make_buffer_rsrc((char*)vc + kvoff, 0, SK * 512, 0x00020000);
  const __amdgpu_buffer_rsrc_t rwq = __builtin_amdgcn_make_buffer_rsrc((void*)wq, 0, DM * 4096 * 2, 0x00020000);
  const __amdgpu_buffer_rsrc_t rwo = __builtin_amdgcn_make_buffer_rsrc((void*)wo, 0, DM * 4096 * 2, 0x00020000);
  const __amdgpu_buffer_rsrc_t rx = __builtin_amdgcn_make_buffer_rsrc((void*)x, 0, DM * 2, 0x00020000);

  const unsigned Eraw = __hip_atomic_load((GAS unsigned*)(ws + WS_E), __ATOMIC_RELAXED, __HIP_MEMORY_SCOPE_AGENT);

  u32x4 WQ[2]; unsigned short xv[2];
  {
    const int row0 = 112 * (s & 3) + 16 * w + (lane >> 3);
    const int oob = (w < 7) ? 0 : 0x40000000;
#pragma unroll
    for (int i = 0; i < 2; ++i) {
      WQ[i] = __builtin_amdgcn_raw_buffer_load_b128(rwq, (row0 + 8 * i) * 8192 + (lane & 7) * 16 + oob, (kvh * 8 + (s >> 2)) * 128, WQ_AUX);
      xv[i] = __builtin_amdgcn_raw_buffer_load_b16(rx, (row0 + 8 * i) * 2 + oob, 0, 0);
    }
  }
  wg_barrier();
  u32x4 K[16], V[16], WO[4], QG[2];
  const int koff = (r >> 1) * 512 + (r & 1) * 64 + g * 16;
  const int voff = g * 1024 + r * 16;
#pragma unroll
  for (int st = 0; st < K1SETS; ++st)
#pragma unroll
    for (int u = 0; u < 4; ++u) K[st * 4 + u] = __builtin_amdgcn_raw_buffer_load_b128(rk, koff + u * 128, SETX(st) * 4096, KV_AUX);
  __builtin_amdgcn_sched_barrier(0);
  const unsigned T = __builtin_amdgcn_readfirstlane(Eraw) + 1u;
  STAMP(1);
  {
    float a[8];
#pragma unroll
    for (int j = 0; j < 8; ++j) a[j] = 0.f;
#pragma unroll
    for (int i = 0; i < 2; ++i) {
      const float xf = bflo(xv[i]);
#pragma unroll
      for (int j = 0; j < 4; ++j) { a[2 * j] = fmaf(xf, bflo(WQ[i][j]), a[2 * j]); a[2 * j + 1] = fmaf(xf, bfhi(WQ[i][j]), a[2 * j + 1]); }
    }
#pragma unroll
    for (int j = 0; j < 8; ++j) { float v = a[j]; v += dpp<XOR8>(v); a[j] = x16_32_sum(v); }
    if (lane < 8) {
      *(f32x4*)(qpart + w * 64 + lane * 8) = f32x4{a[0], a[1], a[2], a[3]};
      *(f32x4*)(qpart + w * 64 + lane * 8 + 4) = f32x4{a[4], a[5], a[6], a[7]};
    }
  }
  wg_barrier();
  if (w == 0 && lane < 32) {
    f32x2 sum = *(const f32x2*)(qpart + lane * 2);
#pragma unroll
    for (int i = 1; i < 7; ++i) sum += *(const f32x2*)(qpart + i * 64 + lane * 2);
    const u32x4 gr = {__float_as_uint(sum[0]), T, __float_as_uint(sum[1]), T};
    __builtin_amdgcn_raw_buffer_store_b128(gr, rws, (int)WS_QG + wg * QG_STRIDE + lane * 16, 0, 16);
  }
  wg_barrier();
  STAMP(2);
#pragma unroll
  for (int st = K1SETS; st < 4; ++st)
#pragma unroll
    for (int u = 0; u < 4; ++u) { PACEW(PK2); K[st * 4 + u] = __builtin_amdgcn_raw_buffer_load_b128(rk, koff + u * 128, SETX(st) * 4096, KV_AUX); }
  const int wooff = (w < 4) ? ((4 * g) * (DM * 2) + r * 16 + w * 256) : 0x7ff00000;
#pragma unroll
  for (int st = 0; st < VPRE; ++st)
#pragma unroll
    for (int u = 0; u < 4; ++u) { PACEW(PK2); V[st * 4 + u] = __builtin_amdgcn_raw_buffer_load_b128(rv, voff + (u >> 1) * 512 + (u & 1) * 256, SETX(st) * 4096, KV_AUX); }
  wg_barrier();
  const int qgoff = (int)WS_QG + (kvh * 32 + w * 4 + (lane >> 5)) * QG_STRIDE + (lane & 31) * 16;
#pragma unroll
  for (int i = 0; i < 2; ++i) QG[i] = __builtin_amdgcn_raw_buffer_load_b128(rws, qgoff + i * 2 * QG_STRIDE, 0, 16);
  wg_barrier();
#pragma unroll
  for (int st = VPRE; st < V1SETS; ++st)
#pragma unroll
    for (int u = 0; u < 4; ++u) { PACEW(PV1); V[st * 4 + u] = __builtin_amdgcn_raw_buffer_load_b128(rv, voff + (u >> 1) * 512 + (u & 1) * 256, SETX(st) * 4096, KV_AUX); }
  __builtin_amdgcn_sched_barrier(0);
  STAMP(3);
  if (!__all(((QG[0][1] ^ T) | (QG[0][3] ^ T) | (QG[1][1] ^ T) | (QG[1][3] ^ T)) == 0u)) {
#pragma unroll 1
    for (unsigned spins = 0; spins < SPIN_CAP; ++spins) {
      asm volatile("" ::: "memory");
#pragma unroll
      for (int i = 0; i < 2; ++i) QG[i] = __builtin_amdgcn_raw_buffer_load_b128(rws, qgoff + i * 2 * QG_STRIDE, 0, 16);
      if (__all(((QG[0][1] ^ T) | (QG[0][3] ^ T) | (QG[1][1] ^ T) | (QG[1][3] ^ T)) == 0u)) break;
      __builtin_amdgcn_s_sleep(POLL_SLEEP);
    }
  }
  {
    const float q0 = x32_sum(__uint_as_float(QG[0][0]) + __uint_as_float(QG[1][0]));
    const float q1 = x32_sum(__uint_as_float(QG[0][2]) + __uint_as_float(QG[1][2]));
    if (lane < 32) qs[w * 32 + lane] = cvtpk(q0, q1);
  }
  STAMP(4);
  wg_barrier();
  const int h = r >> 3, jj = r & 7;
  u32x4 QB[4];
#pragma unroll
  for (int u = 0; u < 4; ++u) QB[u] = *(const u32x4*)((const char*)qs + h * 512 + u * 128 + (r & 1) * 64 + g * 16);
  float sc[8];
#pragma unroll
  for (int st = 0; st < 4; ++st) {
    f32x4 S = {0.f, 0.f, 0.f, 0.f};
#pragma unroll
    for (int u = 0; u < 4; ++u) S = mfma16(K[st * 4 + u], QB[u], S);
    const float a = (r & 1) ? S[1] : S[0], c = (r & 1) ? S[3] : S[2];
    sc[2 * st] = a + dpp<XOR1>(a);
    sc[2 * st + 1] = c + dpp<XOR1>(c);
  }
  float m = fmaxf(fmaxf(fmaxf(sc[0], sc[1]), fmaxf(sc[2], sc[3])), fmaxf(fmaxf(sc[4], sc[5]), fmaxf(sc[6], sc[7])));
  m = x16_32_max(m);
  if ((lane & 0x37) == 0) mx[w * 2 + h] = m;
  wg_barrier();
  float M;
  {
    const float* p = mx + h;
    M = fmaxf(fmaxf(fmaxf(p[0], p[2]), fmaxf(p[4], p[6])), fmaxf(fmaxf(p[8], p[10]), fmaxf(p[12], p[14])));
  }
  float pv[8], l = 0.f;
#pragma unroll
  for (int i = 0; i < 8; ++i) { pv[i] = __builtin_amdgcn_exp2f((sc[i] - M) * C2); l += pv[i]; }
  l = x16_32_sum(l);
  if ((lane & 0x37) == 0) ls[w * 2 + h] = l;
  unsigned msk[4];
#pragma unroll
  for (int i = 0; i < 4; ++i) msk[i] = ((jj >> 1) == i) ? ((jj & 1) ? 0xffff0000u : 0x0000ffffu) : 0u;
  STAMP(5);
#pragma unroll
  for (int st = V1SETS; st < 4; ++st)
#pragma unroll
    for (int u = 0; u < 4; ++u) { PACEW(PV2); V[st * 4 + u] = __builtin_amdgcn_raw_buffer_load_b128(rv, voff + (u >> 1) * 512 + (u & 1) * 256, SETX(st) * 4096, KV_AUX); }
  __builtin_amdgcn_sched_barrier(0);
  wg_barrier();
  float invl, lse;
  {
    const int hp = (t >> 7) & 1;
    float L = 0.f, MM = mx[hp];
#pragma unroll
    for (int i = 0; i < 8; ++i) { L += ls[i * 2 + hp]; MM = fmaxf(MM, mx[i * 2 + hp]); }
    invl = 1.f / L;
    lse = fmaf(MM, C2, __builtin_amdgcn_logf(L));
  }
  f32x4 acc0 = {0.f, 0.f, 0.f, 0.f}, acc1 = {0.f, 0.f, 0.f, 0.f};
#pragma unroll
  for (int st = 0; st < 4; ++st)
#pragma unroll
    for (int i = 0; i < 2; ++i) {
      const unsigned pp = cvtpk(pv[2 * st + i], pv[2 * st + i]);
      const u32x4 A = {pp & msk[0], pp & msk[1], pp & msk[2], pp & msk[3]};
      acc0 = mfma16(A, V[st * 4 + i * 2], acc0);
      acc1 = mfma16(A, V[st * 4 + i * 2 + 1], acc1);
    }
  *(f32x4*)(red + w * 512 + (g >> 1) * 256 + 8 * r + (g & 1) * 4) = acc0;
  *(f32x4*)(red + w * 512 + (g >> 1) * 256 + 128 + 8 * r + (g & 1) * 4) = acc1;
  STAMP(6);
  wg_barrier();
  if (ABL_NOTAIL) { if (red[t] == 12345.678f) out[0] = 1; goto done; }
  {
  if (w < 4) {
    f32x2 pr[8];
#pragma unroll
    for (int i = 0; i < 8; ++i) pr[i] = *(const f32x2*)(red + i * 512 + 2 * t);
    const f32x2 sum = ((pr[0] + pr[1]) + (pr[2] + pr[3])) + ((pr[4] + pr[5]) + (pr[6] + pr[7]));
    const u32x4 gr = {__float_as_uint(sum[0] * invl), T, __float_as_uint(sum[1] * invl), __float_as_uint(lse)};
    __builtin_amdgcn_raw_buffer_store_b128(gr, rws, (int)WS_PG + wg * PG_STRIDE + t * 16, 0, 16);
    __builtin_amdgcn_sched_barrier(0);
    while ((int)((unsigned)rt() - t_start) < TGATE) __builtin_amdgcn_s_sleep(2);
#pragma unroll
    for (int st = 0; st < 4; ++st) WO[st] = __builtin_amdgcn_raw_buffer_load_b128(rwo, wooff + st * (DM * 2), wg * 16 * (DM * 2), WO_AUX);
  }
  STAMP(7);
  if (w == 4) {
    const int pbase = (int)WS_PG + (kvh * 32 + (lane >> 3)) * PG_STRIDE + (s * 8 + (lane & 7)) * 16;
    u32x4 G[4];
    unsigned spins = 0;
    { const unsigned o = (unsigned)rt(); const int lim = max(TA, TAABS - (int)(o - t_start)); while ((int)((unsigned)rt() - o) < lim) __builtin_amdgcn_s_sleep(1); }
#pragma unroll 1
    while (true) {
#pragma unroll
      for (int i = 0; i < 4; ++i) G[i] = __builtin_amdgcn_raw_buffer_load_b128(rws, pbase, i * 8 * PG_STRIDE, 16);
      unsigned bad = 0;
#pragma unroll
      for (int i = 0; i < 4; ++i) bad |= (G[i][1] ^ T) | (G[i][3] ^ dppu<XOR1>(G[i][3])) | (G[i][3] ^ dppu<XOR2>(G[i][3])) | (G[i][3] ^ dppu<XOR7>(G[i][3]));
      if (__all(bad == 0u) || ++spins > SPIN_CAP) break;
      __builtin_amdgcn_s_sleep(POLL_SLEEP);
      asm volatile("" ::: "memory");
    }
    STAMP(8);
    float Mg = fmaxf(fmaxf(__uint_as_float(G[0][3]), __uint_as_float(G[1][3])), fmaxf(__uint_as_float(G[2][3]), __uint_as_float(G[3][3])));
    Mg = fmaxf(Mg, dpp<XOR8>(Mg)); Mg = x16_32_max(Mg);
    float L = 0.f, o0 = 0.f, o1 = 0.f;
#pragma unroll
    for (int i = 0; i < 4; ++i) {
      const float wt = __builtin_amdgcn_exp2f(__uint_as_float(G[i][3]) - Mg);
      L += wt; o0 = fmaf(wt, __uint_as_float(G[i][0]), o0); o1 = fmaf(wt, __uint_as_float(G[i][2]), o1);
    }
    L += dpp<XOR8>(L); o0 += dpp<XOR8>(o0); o1 += dpp<XOR8>(o1);
    L = x16_32_sum(L); o0 = x16_32_sum(o0); o1 = x16_32_sum(o1);
    const float inv = __builtin_amdgcn_rcpf(L);
    o0 *= inv; o1 *= inv;
    if (lane < 8) *(u32x2*)(odup + 2 * lane) = u32x2{cvtpk(o0, o0), cvtpk(o1, o1)};
  }
  wg_barrier();
  if (w < 4) {
    const u32x4 ov = *(const u32x4*)(odup + 4 * g);
    f32x4 yp[4];
#pragma unroll
    for (int st = 0; st < 4; ++st) {
      const u32x4 A = {ov[st] & msk[0], ov[st] & msk[1], ov[st] & msk[2], ov[st] & msk[3]};
      yp[st] = mfma16(A, WO[st], f32x4{0.f, 0.f, 0.f, 0.f});
    }
    const f32x4 ya = (yp[0] + yp[1]) + (yp[2] + yp[3]);
    const float y0 = (g >> 1) ? ya[2] : ya[0], y1 = (g >> 1) ? ya[3] : ya[1];
    const int gi = w * 64 + 4 * r + (g & 1) * 2 + (g >> 1);
    if (gi < 224) {
      const u32x4 gr = {__float_as_uint(y0), T, __float_as_uint(y1), T};
      __builtin_amdgcn_raw_buffer_store_b128(gr, rws, (int)WS_YG + wg * YG_STRIDE + gi * 16, 0, 16);
    }
  }
  STAMP(9);
  if (b < NRED) {
    constexpr int GPP = 224 / NRED, NL = GPP / 2, PPL = 64 / GPP;
    const int c = b;
    const int ybase = (int)WS_YG + (32 * w + lane / GPP) * YG_STRIDE + (c * GPP + (lane % GPP)) * 16;
    u32x4 G[NL];
    unsigned spins = 0;
    { const unsigned o = (unsigned)rt(); while ((int)((unsigned)rt() - o) < TB) __builtin_amdgcn_s_sleep(1); }
#pragma unroll 1
    while (true) {
#pragma unroll
      for (int i = 0; i < NL; ++i) G[i] = __builtin_amdgcn_raw_buffer_load_b128(rws, ybase, i * PPL * YG_STRIDE, 16);
      unsigned bad = 0;
#pragma unroll
      for (int i = 0; i < NL; ++i) bad |= (G[i][1] ^ T) | (G[i][3] ^ T);
      if (__all(bad == 0u) || ++spins > SPIN_CAP) break;
      __builtin_amdgcn_s_sleep(POLL_SLEEP);
      asm volatile("" ::: "memory");
    }
    STAMP(10);
    float y0 = __uint_as_float(G[0][0]), y1 = __uint_as_float(G[0][2]);
#pragma unroll
    for (int i = 1; i < NL; ++i) { y0 += __uint_as_float(G[i][0]); y1 += __uint_as_float(G[i][2]); }
    if (GPP <= 2) { y0 += dpp<XOR2>(y0); y1 += dpp<XOR2>(y1); }
    if (GPP <= 4) { y0 += dpp<ROR4>(y0); y1 += dpp<ROR4>(y1); }
    y0 += dpp<XOR8>(y0); y1 += dpp<XOR8>(y1);
    {
      auto sw = __builtin_amdgcn_permlane16_swap(__float_as_uint(y0), __float_as_uint(y1), false, false);
      y0 = x32_sum(__uint_as_float(sw[0]) + __uint_as_float(sw[1]));
    }
    if ((lane & 47) < GPP) fin[w * 2 * GPP + (lane & 15) * 2 + (lane >> 4)] = y0;
    wg_barrier();
    if (t < GPP) {
      f32x2 pr[8];
#pragma unroll
      for (int i = 0; i < 8; ++i) pr[i] = *(const f32x2*)(fin + i * 2 * GPP + t * 2);
      const f32x2 sum = ((pr[0] + pr[1]) + (pr[2] + pr[3])) + ((pr[4] + pr[5]) + (pr[6] + pr[7]));
      __hip_atomic_store((GAS unsigned*)out + c * GPP + t, cvtpk(sum[0], sum[1]), __ATOMIC_RELAXED, __HIP_MEMORY_SCOPE_AGENT);
      if (b == 0 && t == 0) __hip_atomic_store((GAS unsigned*)(ws + WS_E), T, __ATOMIC_RELAXED, __HIP_MEMORY_SCOPE_AGENT);
    }
  }
  }
done:
  ;
}

extern "C" void kernel_launch(void* const* d_in, const int* in_sizes, int n_in,
                              void* d_out, int out_size, void* d_ws, size_t ws_size,
                              hipStream_t stream) {
  gqa_decode_fused<<<256, NTHR, 0, stream>>>((const u16*)d_in[0], (const u16*)d_in[1], (const u16*)d_in[2], (const u16*)d_in[3],
                                             (const u16*)d_in[4], (u16*)d_out, (char*)d_ws);
}
```

```cpp
#include <hip/hip_runtime.h>
#include <hip/hip_bf16.h>
#include <stdint.h>

typedef unsigned u32x4 __attribute__((ext_vector_type(4)));
typedef unsigned u32x2 __attribute__((ext_vector_type(2)));
typedef float f32x4 __attribute__((ext_vector_type(4)));
typedef float f32x2 __attribute__((ext_vector_type(2)));
typedef __bf16 bf16x8 __attribute__((ext_vector_type(8)));
typedef __bf16 bf16x2 __attribute__((ext_vector_type(2)));
typedef unsigned short u16;
#define GAS __attribute__((address_space(1)))

constexpr int DM = 448, SK = 8192;
constexpr int NW = 8, NTHR = NW * 64;
constexpr float C2 = 0.0625f * 1.4426950408889634f;
constexpr int NT = 2, SC1 = 16;
constexpr int OOB = 0x7ff00000;
constexpr unsigned SPIN_CAP = 1u << 18;
constexpr int DELAY_A = 70, DELAY_B = 30;

constexpr int HP_A = 0;
constexpr int HP_B = 1 << 20;
constexpr int HP_PROBE = HP_B + 224 * 4096;
constexpr int WS_QG = 2 << 20;
constexpr int QG_STRIDE = 512;
constexpr int WS_PR = (2 << 20) + 262144;

__device__ __forceinline__ unsigned cvtpk(float lo, float hi) { f32x2 v = {lo, hi}; return __builtin_bit_cast(unsigned, __builtin_convertvector(v, bf16x2)); }
__device__ __forceinline__ float bflo(unsigned u) { return __uint_as_float(u << 16); }
__device__ __forceinline__ float bfhi(unsigned u) { return __uint_as_float(u & 0xffff0000u); }
__device__ __forceinline__ f32x4 mfma16(u32x4 a, u32x4 b, f32x4 c) {
  return __builtin_amdgcn_mfma_f32_16x16x32_bf16(__builtin_bit_cast(bf16x8, a), __builtin_bit_cast(bf16x8, b), c, 0, 0, 0);
}
template <int CTRL> __device__ __forceinline__ float dpp(float x) {
  return __builtin_bit_cast(float, __builtin_amdgcn_mov_dpp(__builtin_bit_cast(int, x), CTRL, 0xf, 0xf, true));
}
template <int CTRL> __device__ __forceinline__ unsigned dppu(unsigned x) {
  return (unsigned)__builtin_amdgcn_mov_dpp((int)x, CTRL, 0xf, 0xf, true);
}
constexpr int XOR1 = 0xB1, XOR2 = 0x4E, XOR7 = 0x141, ROR8 = 0x128, ROR4 = 0x124;
__device__ __forceinline__ float x32_sum(float x) {
  auto t = __builtin_amdgcn_permlane32_swap(__float_as_uint(x), __float_as_uint(x), false, false);
  return __uint_as_float(t[0]) + __uint_as_float(t[1]);
}
__device__ __forceinline__ float x16_32_sum(float x) {
  auto s = __builtin_amdgcn_permlane16_swap(__float_as_uint(x), __float_as_uint(x), false, false);
  return x32_sum(__uint_as_float(s[0]) + __uint_as_float(s[1]));
}
__device__ __forceinline__ float x16_32_max(float x) {
  auto s = __builtin_amdgcn_permlane16_swap(__float_as_uint(x), __float_as_uint(x), false, false);
  x = fmaxf(__uint_as_float(s[0]), __uint_as_float(s[1]));
  auto t = __builtin_amdgcn_permlane32_swap(__float_as_uint(x), __float_as_uint(x), false, false);
  return fmaxf(__uint_as_float(t[0]), __uint_as_float(t[1]));
}
__device__ __forceinline__ void wg_barrier() { asm volatile("s_waitcnt lgkmcnt(0)\n\ts_barrier" ::: "memory"); }
__device__ __forceinline__ void wait_ticks(int n) {
  const unsigned t0 = (unsigned)__builtin_amdgcn_s_memrealtime();
  while ((int)((unsigned)__builtin_amdgcn_s_memrealtime() - t0) < n) __builtin_amdgcn_s_sleep(1);
}
#define LD128(rs, voff, soff, aux) __builtin_amdgcn_raw_buffer_load_b128(rs, voff, soff, aux)
#define ST128(v, rs, voff) __builtin_amdgcn_raw_buffer_store_b128(v, rs, voff, 0, SC1)

__global__ void __launch_bounds__(NTHR, 2)
gqa_decode_fused(const u16* __restrict__ x, const u16* __restrict__ kc, const u16* __restrict__ vc, const u16* __restrict__ wq,
                 const u16* __restrict__ wo, u16* out, char* ws) {
  __shared__ __attribute__((aligned(16))) float lds[4096 + 256 + 512 + 16 + 16 + 16 + 128 + 4 + 16000];
  float* red = lds;
  unsigned* qs = (unsigned*)(lds + 4096);
  float* qpart = lds + 4096 + 256;
  float* mx = qpart + 512;
  float* ls = mx + 16;
  unsigned* odup = (unsigned*)(ls + 16);
  float* fin = ls + 32;
  unsigned* sidew = (unsigned*)(fin + 128);

  const int kvh = blockIdx.x & 7, s = blockIdx.x >> 3;
  const int wg = kvh * 32 + s;
  const int t = threadIdx.x, lane = t & 63, w = __builtin_amdgcn_readfirstlane(t >> 6), g = lane >> 4, r = lane & 15;

  char* hp = (char*)(((size_t)ws + 4096 + 0x1fffff) & ~(size_t)0x1fffff);
  const __amdgpu_buffer_rsrc_t rws = __builtin_amdgcn_make_buffer_rsrc(hp, 0, 4u << 20, 0x00020000);
  const size_t kvoff = ((size_t)kvh * SK + s * 256) * 512;
  const __amdgpu_buffer_rsrc_t rk = __builtin_amdgcn_make_buffer_rsrc((char*)kc + kvoff, 0, 131072, 0x00020000);
  const __amdgpu_buffer_rsrc_t rv = __builtin_amdgcn_make_buffer_rsrc((char*)vc + kvoff, 0, 131072, 0x00020000);
  const __amdgpu_buffer_rsrc_t rwq = __builtin_amdgcn_make_buffer_rsrc((void*)wq, 0, DM * 4096 * 2, 0x00020000);
  const __amdgpu_buffer_rsrc_t rwo = __builtin_amdgcn_make_buffer_rsrc((void*)wo, 0, DM * 4096 * 2, 0x00020000);
  const __amdgpu_buffer_rsrc_t rx = __builtin_amdgcn_make_buffer_rsrc((void*)x, 0, DM * 2, 0x00020000);

  unsigned long long pc0 = 0;
  {
    const int pl = kvh * 16 + (s >> 1);
    const int poff = (t == 0) ? HP_PROBE + (pl >> 6) * 32768 + (s & 1) * 8192 + (pl & 63) * 128 : OOB;
    if (w == 0) asm volatile("s_memtime %0\n\ts_waitcnt lgkmcnt(0)" : "=s"(pc0) :: "memory");
    __builtin_amdgcn_raw_buffer_store_b32(0u, rws, poff, 0, SC1);
  }
  const unsigned Eraw = __hip_atomic_load((GAS unsigned*)ws, __ATOMIC_RELAXED, __HIP_MEMORY_SCOPE_AGENT);

  u32x4 WQ[2]; unsigned short xv[2];
  {
    const int row0 = 112 * (s & 3) + 16 * (w - 1) + (lane >> 3);
    const int oob = (w > 0) ? 0 : 0x40000000;
#pragma unroll
    for (int i = 0; i < 2; ++i) {
      WQ[i] = LD128(rwq, (row0 + 8 * i) * 8192 + (lane & 7) * 16 + oob, (kvh * 8 + (s >> 2)) * 128, NT);
      xv[i] = __builtin_amdgcn_raw_buffer_load_b16(rx, (row0 + 8 * i) * 2 + oob, 0, 0);
    }
  }
  wg_barrier();
  unsigned plat = 0;
  if (w == 0) {
    unsigned long long pc1;
    asm volatile("s_waitcnt vmcnt(5)\n\ts_memtime %0\n\ts_waitcnt lgkmcnt(0)" : "=s"(pc1) :: "memory");
    plat = min((unsigned)(pc1 - pc0), 4000u);
  }

  u32x4 K[16], V[16], WO[4], QG[2];
  const int koff = (r >> 1) * 512 + (r & 1) * 64 + g * 16;
  const int voff = g * 1024 + r * 16;
#define LOADK(st) _Pragma("unroll") for (int u = 0; u < 4; ++u) K[(st) * 4 + u] = LD128(rk, koff + u * 128, ((st) * 8 + w) * 4096, NT)
#define LOADV(st) _Pragma("unroll") for (int u = 0; u < 4; ++u) V[(st) * 4 + u] = LD128(rv, voff + (u >> 1) * 512 + (u & 1) * 256, ((st) * 8 + w) * 4096, NT)
  LOADK(0); LOADK(1);
  __builtin_amdgcn_sched_barrier(0);
  const unsigned T = __builtin_amdgcn_readfirstlane(Eraw) + 1u;
  {
    float a[8];
#pragma unroll
    for (int j = 0; j < 8; ++j) a[j] = 0.f;
#pragma unroll
    for (int i = 0; i < 2; ++i) {
      const float xf = bflo(xv[i]);
#pragma unroll
      for (int j = 0; j < 4; ++j) { a[2 * j] = fmaf(xf, bflo(WQ[i][j]), a[2 * j]); a[2 * j + 1] = fmaf(xf, bfhi(WQ[i][j]), a[2 * j + 1]); }
    }
#pragma unroll
    for (int j = 0; j < 8; ++j) { float v = a[j]; v += dpp<ROR8>(v); a[j] = x16_32_sum(v); }
    if (lane < 8) {
      *(f32x4*)(qpart + w * 64 + lane * 8) = f32x4{a[0], a[1], a[2], a[3]};
      *(f32x4*)(qpart + w * 64 + lane * 8 + 4) = f32x4{a[4], a[5], a[6], a[7]};
    }
  }
  wg_barrier();
  if (w == 0) {
    if (lane < 32) {
      f32x2 sum = *(const f32x2*)(qpart + 64 + lane * 2);
#pragma unroll
      for (int i = 2; i < 8; ++i) sum += *(const f32x2*)(qpart + i * 64 + lane * 2);
      const u32x4 gr = {__float_as_uint(sum[0]), T, __float_as_uint(sum[1]), T};
      ST128(gr, rws, WS_QG + wg * QG_STRIDE + lane * 16);
    } else if (lane == 32) {
      const u32x4 gr = {plat, T, plat, T};
      ST128(gr, rws, WS_PR + wg * 16);
    }
  }
  wg_barrier();
  LOADK(2); LOADK(3); LOADV(0);
  wg_barrier();
  const int qgoff = WS_QG + (kvh * 32 + w * 4 + (lane >> 5)) * QG_STRIDE + (lane & 31) * 16;
  const int proff = (w == 0) ? WS_PR + ((kvh & 3) * 32 + (lane >> 5) * 128 + (lane & 31)) * 16 : OOB;
  const unsigned Tpr = (w == 0) ? T : 0u;
#pragma unroll
  for (int i = 0; i < 2; ++i) QG[i] = LD128(rws, qgoff + i * 2 * QG_STRIDE, 0, SC1);
  u32x4 PR = LD128(rws, proff, 0, SC1);
  wg_barrier();
  LOADV(1);
  __builtin_amdgcn_sched_barrier(0);
  if (!__all(((QG[0][1] ^ T) | (QG[0][3] ^ T) | (QG[1][1] ^ T) | (QG[1][3] ^ T) | (PR[1] ^ Tpr) | (PR[3] ^ Tpr)) == 0u)) {
#pragma unroll 1
    for (unsigned spins = 0; spins < SPIN_CAP; ++spins) {
      asm volatile("" ::: "memory");
#pragma unroll
      for (int i = 0; i < 2; ++i) QG[i] = LD128(rws, qgoff + i * 2 * QG_STRIDE, 0, SC1);
      PR = LD128(rws, proff, 0, SC1);
      if (__all(((QG[0][1] ^ T) | (QG[0][3] ^ T) | (QG[1][1] ^ T) | (QG[1][3] ^ T) | (PR[1] ^ Tpr) | (PR[3] ^ Tpr)) == 0u)) break;
      __builtin_amdgcn_s_sleep(1);
    }
  }
  {
    const float q0 = x32_sum(__uint_as_float(QG[0][0]) + __uint_as_float(QG[1][0]));
    const float q1 = x32_sum(__uint_as_float(QG[0][2]) + __uint_as_float(QG[1][2]));
    if (lane < 32) qs[w * 32 + lane] = cvtpk(q0, q1);
    if (w == 0) {
      int d = (((lane ^ (lane >> 5)) & 1) ? 1 : -1) * (int)PR[0];
      d += (int)dppu<XOR1>((unsigned)d); d += (int)dppu<XOR2>((unsigned)d); d += (int)dppu<ROR4>((unsigned)d); d += (int)dppu<ROR8>((unsigned)d);
      d = __builtin_amdgcn_readlane(d, 0) + __builtin_amdgcn_readlane(d, 16) + __builtin_amdgcn_readlane(d, 32) + __builtin_amdgcn_readlane(d, 48);
      if (lane == 0) sidew[0] = (d < 0 ? 1u : 0u) ^ (unsigned)(kvh >> 2);
    }
  }
  wg_barrier();
  const int nb = __builtin_amdgcn_readfirstlane(sidew[0]);

  const int h = r >> 3, jj = r & 7;
  u32x4 QB[4];
#pragma unroll
  for (int u = 0; u < 4; ++u) QB[u] = *(const u32x4*)((const char*)qs + h * 512 + u * 128 + (r & 1) * 64 + g * 16);
  float sc[8];
#pragma unroll
  for (int st = 0; st < 4; ++st) {
    f32x4 S = {0.f, 0.f, 0.f, 0.f};
#pragma unroll
    for (int u = 0; u < 4; ++u) S = mfma16(K[st * 4 + u], QB[u], S);
    const float a = (r & 1) ? S[1] : S[0], c = (r & 1) ? S[3] : S[2];
    const unsigned sp = cvtpk(a + dpp<XOR1>(a), c + dpp<XOR1>(c));
    sc[2 * st] = bflo(sp);
    sc[2 * st + 1] = bfhi(sp);
  }
  float m = fmaxf(fmaxf(fmaxf(sc[0], sc[1]), fmaxf(sc[2], sc[3])), fmaxf(fmaxf(sc[4], sc[5]), fmaxf(sc[6], sc[7])));
  m = x16_32_max(m);
  if ((lane & 0x37) == 0) mx[w * 2 + h] = m;
  wg_barrier();
  float M;
  {
    const float* p = mx + h;
    M = fmaxf(fmaxf(fmaxf(p[0], p[2]), fmaxf(p[4], p[6])), fmaxf(fmaxf(p[8], p[10]), fmaxf(p[12], p[14])));
  }
  float pv[8], l = 0.f;
#pragma unroll
  for (int i = 0; i < 8; ++i) { pv[i] = __builtin_amdgcn_exp2f((sc[i] - M) * C2); l += pv[i]; }
  l = x16_32_sum(l);
  if ((lane & 0x37) == 0) ls[w * 2 + h] = l;
  unsigned msk[4];
#pragma unroll
  for (int i = 0; i < 4; ++i) msk[i] = ((jj >> 1) == i) ? ((jj & 1) ? 0xffff0000u : 0x0000ffffu) : 0u;
  LOADV(2); LOADV(3);
  __builtin_amdgcn_sched_barrier(0);
  wg_barrier();
  float invl, lse;
  int pgoff, ygoff;
  {
    const int hp2 = (t >> 7) & 1;
    float L = 0.f, MM = mx[hp2];
#pragma unroll
    for (int i = 0; i < 8; ++i) { L += ls[i * 2 + hp2]; MM = fmaxf(MM, mx[i * 2 + hp2]); }
    invl = __builtin_amdgcn_rcpf(L);
    lse = fmaf(MM, C2, __builtin_amdgcn_logf(L));
    const int c = (t >> 3) & 31;
    pgoff = HP_A + (kvh & 3) * 262144 + ((c >> 1) * 4 + nb * 2 + (c & 1)) * 4096 + s * 128 + (t & 7) * 16;
    const int gi = w * 64 + 4 * r + (g & 1) * 2 + (g >> 1), cb = gi >> 3, mb = (cb >> 1) * 8 + kvh;
    ygoff = (gi < 224) ? HP_B + ((mb >> 1) * 4 + (cb & 1) * 2 + (mb & 1)) * 4096 + s * 128 + (gi & 7) * 16 : OOB;
  }
  asm volatile("" : "+v"(invl), "+v"(lse), "+v"(pgoff), "+v"(ygoff));

  f32x4 acc0 = {0.f, 0.f, 0.f, 0.f}, acc1 = {0.f, 0.f, 0.f, 0.f};
#pragma unroll
  for (int st = 0; st < 4; ++st)
#pragma unroll
    for (int i = 0; i < 2; ++i) {
      const unsigned pp = cvtpk(pv[2 * st + i], pv[2 * st + i]);
      const u32x4 A = {pp & msk[0], pp & msk[1], pp & msk[2], pp & msk[3]};
      acc0 = mfma16(A, V[st * 4 + i * 2], acc0);
      acc1 = mfma16(A, V[st * 4 + i * 2 + 1], acc1);
    }
  *(f32x4*)(red + w * 512 + (g >> 1) * 256 + 8 * r + (g & 1) * 4) = acc0;
  *(f32x4*)(red + w * 512 + (g >> 1) * 256 + 128 + 8 * r + (g & 1) * 4) = acc1;
  wg_barrier();
  if (w < 4) {
    f32x2 pr[8];
#pragma unroll
    for (int i = 0; i < 8; ++i) pr[i] = *(const f32x2*)(red + i * 512 + 2 * t);
    const f32x2 sum = ((pr[0] + pr[1]) + (pr[2] + pr[3])) + ((pr[4] + pr[5]) + (pr[6] + pr[7]));
    const u32x4 gr = {__float_as_uint(sum[0] * invl), T, __float_as_uint(sum[1] * invl), __float_as_uint(lse)};
    ST128(gr, rws, pgoff);
    __builtin_amdgcn_sched_barrier(0);
    const int wooff = (4 * g) * (DM * 2) + r * 16 + w * 256;
#pragma unroll
    for (int st = 0; st < 4; ++st) WO[st] = LD128(rwo, wooff + st * (DM * 2), wg * 16 * (DM * 2), NT);
  }
  if (w == 4) {
    const int pbase = HP_A + (kvh & 3) * 262144 + ((s >> 1) * 4 + nb * 2 + (s & 1)) * 4096 + lane * 16;
    u32x4 G[4];
    unsigned spins = 0;
    wait_ticks(DELAY_A);
#pragma unroll 1
    while (true) {
#pragma unroll
      for (int i = 0; i < 4; ++i) G[i] = LD128(rws, pbase + i * 1024, 0, SC1);
      unsigned bad = 0;
#pragma unroll
      for (int i = 0; i < 4; ++i) bad |= (G[i][1] ^ T) | (G[i][3] ^ dppu<XOR1>(G[i][3])) | (G[i][3] ^ dppu<XOR2>(G[i][3])) | (G[i][3] ^ dppu<XOR7>(G[i][3]));
      if (__all(bad == 0u) || ++spins > SPIN_CAP) break;
      __builtin_amdgcn_s_sleep(1);
      asm volatile("" ::: "memory");
    }
    float Mg = fmaxf(fmaxf(__uint_as_float(G[0][3]), __uint_as_float(G[1][3])), fmaxf(__uint_as_float(G[2][3]), __uint_as_float(G[3][3])));
    Mg = fmaxf(Mg, dpp<ROR8>(Mg)); Mg = x16_32_max(Mg);
    float L = 0.f, o0 = 0.f, o1 = 0.f;
#pragma unroll
    for (int i = 0; i < 4; ++i) {
      const float wt = __builtin_amdgcn_exp2f(__uint_as_float(G[i][3]) - Mg);
      L += wt; o0 = fmaf(wt, __uint_as_float(G[i][0]), o0); o1 = fmaf(wt, __uint_as_float(G[i][2]), o1);
    }
    L += dpp<ROR8>(L); o0 += dpp<ROR8>(o0); o1 += dpp<ROR8>(o1);
    L = x16_32_sum(L); o0 = x16_32_sum(o0); o1 = x16_32_sum(o1);
    const float inv = __builtin_amdgcn_rcpf(L);
    o0 *= inv; o1 *= inv;
    if (lane < 8) *(u32x2*)(odup + 2 * lane) = u32x2{cvtpk(o0, o0), cvtpk(o1, o1)};
  }
  wg_barrier();
  if (w < 4) {
    const u32x4 ov = *(const u32x4*)(odup + 4 * g);
    f32x4 yp[4];
#pragma unroll
    for (int st = 0; st < 4; ++st) {
      const u32x4 A = {ov[st] & msk[0], ov[st] & msk[1], ov[st] & msk[2], ov[st] & msk[3]};
      yp[st] = mfma16(A, WO[st], f32x4{0.f, 0.f, 0.f, 0.f});
    }
    const f32x4 ya = (yp[0] + yp[1]) + (yp[2] + yp[3]);
    const float y0 = (g >> 1) ? ya[2] : ya[0], y1 = (g >> 1) ? ya[3] : ya[1];
    const u32x4 gr = {__float_as_uint(y0), T, __float_as_uint(y1), T};
    ST128(gr, rws, ygoff);
  }
  const int kred = s * 4 + (kvh & 3);
  if (kred < 14) {
    const int c = kred * 2 + nb, mb = kred * 8 + w;
    const int ybase = HP_B + ((mb >> 1) * 4 + nb * 2 + (mb & 1)) * 4096 + lane * 16;
    u32x4 G[4];
    unsigned spins = 0;
    wait_ticks(DELAY_B);
#pragma unroll 1
    while (true) {
#pragma unroll
      for (int i = 0; i < 4; ++i) G[i] = LD128(rws, ybase + i * 1024, 0, SC1);
      unsigned bad = 0;
#pragma unroll
      for (int i = 0; i < 4; ++i) bad |= (G[i][1] ^ T) | (G[i][3] ^ T);
      if (__all(bad == 0u) || ++spins > SPIN_CAP) break;
      __builtin_amdgcn_s_sleep(1);
      asm volatile("" ::: "memory");
    }
    float y0 = (__uint_as_float(G[0][0]) + __uint_as_float(G[1][0])) + (__uint_as_float(G[2][0]) + __uint_as_float(G[3][0]));
    float y1 = (__uint_as_float(G[0][2]) + __uint_as_float(G[1][2])) + (__uint_as_float(G[2][2]) + __uint_as_float(G[3][2]));
    y0 += dpp<ROR8>(y0); y1 += dpp<ROR8>(y1);
    {
      auto sw = __builtin_amdgcn_permlane16_swap(__float_as_uint(y0), __float_as_uint(y1), false, false);
      y0 = x32_sum(__uint_as_float(sw[0]) + __uint_as_float(sw[1]));
    }
    if ((lane & 40) == 0) fin[w * 16 + (lane & 7) * 2 + (lane >> 4)] = y0;
    wg_barrier();
    if (t < 8) {
      f32x2 pr[8];
#pragma unroll
      for (int i = 0; i < 8; ++i) pr[i] = *(const f32x2*)(fin + i * 16 + t * 2);
      const f32x2 sum = ((pr[0] + pr[1]) + (pr[2] + pr[3])) + ((pr[4] + pr[5]) + (pr[6] + pr[7]));
      __hip_atomic_store((GAS unsigned*)out + c * 8 + t, cvtpk(sum[0], sum[1]), __ATOMIC_RELAXED, __HIP_MEMORY_SCOPE_AGENT);
      if (wg == 0 && t == 0) __hip_atomic_store((GAS unsigned*)ws, T, __ATOMIC_RELAXED, __HIP_MEMORY_SCOPE_AGENT);
    }
  }
}

extern "C" void kernel_launch(void* const* d_in, const int* in_sizes, int n_in,
                              void* d_out, int out_size, void* d_ws, size_t ws_size,
                              hipStream_t stream) {
  gqa_decode_fused<<<256, NTHR, 0, stream>>>((const u16*)d_in[0], (const u16*)d_in[1], (const u16*)d_in[2], (const u16*)d_in[3],
                                             (const u16*)d_in[4], (u16*)d_out, (char*)d_ws);
}
```

```cpp
#include <hip/hip_runtime.h>
#include <hip/hip_bf16.h>
#include <stdint.h>

typedef unsigned u32x4 __attribute__((ext_vector_type(4)));
typedef unsigned u32x2 __attribute__((ext_vector_type(2)));
typedef float f32x4 __attribute__((ext_vector_type(4)));
typedef float f32x2 __attribute__((ext_vector_type(2)));
typedef __bf16 bf16x8 __attribute__((ext_vector_type(8)));
typedef __bf16 bf16x2 __attribute__((ext_vector_type(2)));
typedef unsigned short u16;
#define GAS __attribute__((address_space(1)))

constexpr int DM = 448, SK = 8192;
constexpr int NW = 8, NTHR = NW * 64;
constexpr float C2 = 0.0625f * 1.4426950408889634f;
constexpr int NT = 2, SC1 = 16;
constexpr int OOB = 0x7ff00000;
constexpr unsigned SPIN_CAP = 1u << 18;
constexpr int DELAY_A = 70, DELAY_B = 30;

constexpr int HP_A = 0;
constexpr int HP_B = 1 << 20;
constexpr int HP_PROBE = HP_B + 224 * 4096;
constexpr int WS_QG = 2 << 20;
constexpr int QG_STRIDE = 512;
constexpr int WS_PR = (2 << 20) + 262144;

__device__ __forceinline__ unsigned cvtpk(float lo, float hi) { f32x2 v = {lo, hi}; return __builtin_bit_cast(unsigned, __builtin_convertvector(v, bf16x2)); }
__device__ __forceinline__ float bflo(unsigned u) { return __uint_as_float(u << 16); }
__device__ __forceinline__ float bfhi(unsigned u) { return __uint_as_float(u & 0xffff0000u); }
__device__ __forceinline__ f32x4 mfma16(u32x4 a, u32x4 b, f32x4 c) {
  return __builtin_amdgcn_mfma_f32_16x16x32_bf16(__builtin_bit_cast(bf16x8, a), __builtin_bit_cast(bf16x8, b), c, 0, 0, 0);
}
template <int CTRL> __device__ __forceinline__ float dpp(float x) {
  return __builtin_bit_cast(float, __builtin_amdgcn_mov_dpp(__builtin_bit_cast(int, x), CTRL, 0xf, 0xf, true));
}
template <int CTRL> __device__ __forceinline__ unsigned dppu(unsigned x) {
  return (unsigned)__builtin_amdgcn_mov_dpp((int)x, CTRL, 0xf, 0xf, true);
}
constexpr int XOR1 = 0xB1, XOR2 = 0x4E, XOR7 = 0x141, ROR8 = 0x128, ROR4 = 0x124;
__device__ __forceinline__ float x32_sum(float x) {
  auto t = __builtin_amdgcn_permlane32_swap(__float_as_uint(x), __float_as_uint(x), false, false);
  return __uint_as_float(t[0]) + __uint_as_float(t[1]);
}
__device__ __forceinline__ float x16_32_sum(float x) {
  auto s = __builtin_amdgcn_permlane16_swap(__float_as_uint(x), __float_as_uint(x), false, false);
  return x32_sum(__uint_as_float(s[0]) + __uint_as_float(s[1]));
}
__device__ __forceinline__ float x16_32_max(float x) {
  auto s = __builtin_amdgcn_permlane16_swap(__float_as_uint(x), __float_as_uint(x), false, false);
  x = fmaxf(__uint_as_float(s[0]), __uint_as_float(s[1]));
  auto t = __builtin_amdgcn_permlane32_swap(__float_as_uint(x), __float_as_uint(x), false, false);
  return fmaxf(__uint_as_float(t[0]), __uint_as_float(t[1]));
}
__device__ __forceinline__ void wg_barrier() { asm volatile("s_waitcnt lgkmcnt(0)\n\ts_barrier" ::: "memory"); }
__device__ __forceinline__ void wait_ticks(int n) {
  const unsigned t0 = (unsigned)__builtin_amdgcn_s_memrealtime();
  while ((int)((unsigned)__builtin_amdgcn_s_memrealtime() - t0) < n) __builtin_amdgcn_s_sleep(1);
}
#define LD128(rs, voff, soff, aux) __builtin_amdgcn_raw_buffer_load_b128(rs, voff, soff, aux)
#define ST128(v, rs, voff) __builtin_amdgcn_raw_buffer_store_b128(v, rs, voff, 0, SC1)

__global__ void __launch_bounds__(NTHR, 2)
gqa_decode_fused(const u16* __restrict__ x, const u16* __restrict__ kc, const u16* __restrict__ vc, const u16* __restrict__ wq,
                 const u16* __restrict__ wo, u16* out, char* ws) {
  __shared__ __attribute__((aligned(16))) float lds[4096 + 256 + 512 + 16 + 16 + 16 + 128 + 4 + 16000];
  float* red = lds;
  unsigned* qs = (unsigned*)(lds + 4096);
  float* qpart = lds + 4096 + 256;
  float* mx = qpart + 512;
  float* ls = mx + 16;
  unsigned* odup = (unsigned*)(ls + 16);
  float* fin = ls + 32;
  unsigned* sidew = (unsigned*)(fin + 128);

  const int kvh = blockIdx.x & 7, s = blockIdx.x >> 3;
  const int wg = kvh * 32 + s;
  const int t = threadIdx.x, lane = t & 63, w = __builtin_amdgcn_readfirstlane(t >> 6), g = lane >> 4, r = lane & 15;

  char* hp = (char*)(((size_t)ws + 4096 + 0x1fffff) & ~(size_t)0x1fffff);
  const __amdgpu_buffer_rsrc_t rws = __builtin_amdgcn_make_buffer_rsrc(hp, 0, 4u << 20, 0x00020000);
  const size_t kvoff = ((size_t)kvh * SK + s * 256) * 512;
  const __amdgpu_buffer_rsrc_t rk = __builtin_amdgcn_make_buffer_rsrc((char*)kc + kvoff, 0, 131072, 0x00020000);
  const __amdgpu_buffer_rsrc_t rv = __builtin_amdgcn_make_buffer_rsrc((char*)vc + kvoff, 0, 131072, 0x00020000);
  const __amdgpu_buffer_rsrc_t rwq = __builtin_amdgcn_make_buffer_rsrc((void*)wq, 0, DM * 4096 * 2, 0x00020000);
  const __amdgpu_buffer_rsrc_t rwo = __builtin_amdgcn_make_buffer_rsrc((void*)wo, 0, DM * 4096 * 2, 0x00020000);
  const __amdgpu_buffer_rsrc_t rx = __builtin_amdgcn_make_buffer_rsrc((void*)x, 0, DM * 2, 0x00020000);

  {
    const int pl = kvh * 16 + (s >> 1);
    const int pbase = HP_PROBE + (pl >> 6) * 32768 + (s & 1) * 65536 + (pl & 63) * 128;
    __builtin_amdgcn_raw_buffer_store_b32(0u, rws, (t == 0) ? pbase + (s & 1) * 8192 : OOB, 0, SC1);
    __builtin_amdgcn_raw_buffer_store_b32(0u, rws, (t == 0) ? pbase + ((s & 1) ^ 1) * 8192 : OOB, 0, SC1);
  }
  const unsigned Eraw = __hip_atomic_load((GAS unsigned*)ws, __ATOMIC_RELAXED, __HIP_MEMORY_SCOPE_AGENT);

  u32x4 WQ[2]; unsigned short xv[2];
  {
    const int row0 = 112 * (s & 3) + 16 * (w - 1) + (lane >> 3);
    const int oob = (w > 0) ? 0 : 0x40000000;
#pragma unroll
    for (int i = 0; i < 2; ++i) {
      WQ[i] = LD128(rwq, (row0 + 8 * i) * 8192 + (lane & 7) * 16 + oob, (kvh * 8 + (s >> 2)) * 128, NT);
      xv[i] = __builtin_amdgcn_raw_buffer_load_b16(rx, (row0 + 8 * i) * 2 + oob, 0, 0);
    }
  }
  wg_barrier();
  unsigned plat = 0;
  if (w == 0) {
    unsigned long long pc1, pc2;
    asm volatile("s_waitcnt vmcnt(6)\n\ts_memtime %0\n\ts_waitcnt lgkmcnt(0) vmcnt(5)\n\ts_memtime %1\n\ts_waitcnt lgkmcnt(0)" : "=&s"(pc1), "=&s"(pc2) :: "memory");
    plat = min((unsigned)(pc2 - pc1), 2000u);
  }

  u32x4 K[16], V[16], WO[4], QG[2];
  const int koff = (r >> 1) * 512 + (r & 1) * 64 + g * 16;
  const int voff = g * 1024 + r * 16;
#define LOADK(st) _Pragma("unroll") for (int u = 0; u < 4; ++u) K[(st) * 4 + u] = LD128(rk, koff + u * 128, ((st) * 8 + w) * 4096, NT)
#define LOADV(st) _Pragma("unroll") for (int u = 0; u < 4; ++u) V[(st) * 4 + u] = LD128(rv, voff + (u >> 1) * 512 + (u & 1) * 256, ((st) * 8 + w) * 4096, NT)
  LOADK(0); LOADK(1);
  __builtin_amdgcn_sched_barrier(0);
  const unsigned T = __builtin_amdgcn_readfirstlane(Eraw) + 1u;
  {
    float a[8];
#pragma unroll
    for (int j = 0; j < 8; ++j) a[j] = 0.f;
#pragma unroll
    for (int i = 0; i < 2; ++i) {
      const float xf = bflo(xv[i]);
#pragma unroll
      for (int j = 0; j < 4; ++j) { a[2 * j] = fmaf(xf, bflo(WQ[i][j]), a[2 * j]); a[2 * j + 1] = fmaf(xf, bfhi(WQ[i][j]), a[2 * j + 1]); }
    }
#pragma unroll
    for (int j = 0; j < 8; ++j) { float v = a[j]; v += dpp<ROR8>(v); a[j] = x16_32_sum(v); }
    if (lane < 8) {
      *(f32x4*)(qpart + w * 64 + lane * 8) = f32x4{a[0], a[1], a[2], a[3]};
      *(f32x4*)(qpart + w * 64 + lane * 8 + 4) = f32x4{a[4], a[5], a[6], a[7]};
    }
  }
  wg_barrier();
  if (w == 0) {
    if (lane < 32) {
      f32x2 sum = *(const f32x2*)(qpart + 64 + lane * 2);
#pragma unroll
      for (int i = 2; i < 8; ++i) sum += *(const f32x2*)(qpart + i * 64 + lane * 2);
      const u32x4 gr = {__float_as_uint(sum[0]), T, __float_as_uint(sum[1]), T};
      ST128(gr, rws, WS_QG + wg * QG_STRIDE + lane * 16);
    } else if (lane == 32) {
      const u32x4 gr = {plat, T, plat, T};
      ST128(gr, rws, WS_PR + wg * 16);
    }
  }
  wg_barrier();
  LOADK(2); LOADK(3); LOADV(0);
  wg_barrier();
  const int qgoff = WS_QG + (kvh * 32 + w * 4 + (lane >> 5)) * QG_STRIDE + (lane & 31) * 16;
  const int proff = (w == 0) ? WS_PR + ((kvh & 3) * 32 + (lane >> 5) * 128 + (lane & 31)) * 16 : OOB;
  const unsigned Tpr = (w == 0) ? T : 0u;
#pragma unroll
  for (int i = 0; i < 2; ++i) QG[i] = LD128(rws, qgoff + i * 2 * QG_STRIDE, 0, SC1);
  u32x4 PR = LD128(rws, proff, 0, SC1);
  wg_barrier();
  LOADV(1);
  __builtin_amdgcn_sched_barrier(0);
  if (!__all(((QG[0][1] ^ T) | (QG[0][3] ^ T) | (QG[1][1] ^ T) | (QG[1][3] ^ T) | (PR[1] ^ Tpr) | (PR[3] ^ Tpr)) == 0u)) {
#pragma unroll 1
    for (unsigned spins = 0; spins < SPIN_CAP; ++spins) {
      asm volatile("" ::: "memory");
#pragma unroll
      for (int i = 0; i < 2; ++i) QG[i] = LD128(rws, qgoff + i * 2 * QG_STRIDE, 0, SC1);
      PR = LD128(rws, proff, 0, SC1);
      if (__all(((QG[0][1] ^ T) | (QG[0][3] ^ T) | (QG[1][1] ^ T) | (QG[1][3] ^ T) | (PR[1] ^ Tpr) | (PR[3] ^ Tpr)) == 0u)) break;
      __builtin_amdgcn_s_sleep(1);
    }
  }
  {
    const float q0 = x32_sum(__uint_as_float(QG[0][0]) + __uint_as_float(QG[1][0]));
    const float q1 = x32_sum(__uint_as_float(QG[0][2]) + __uint_as_float(QG[1][2]));
    if (lane < 32) qs[w * 32 + lane] = cvtpk(q0, q1);
    if (w == 0) {
      int d = (((lane ^ (lane >> 5)) & 1) ? 1 : -1) * (int)PR[0];
      d += (int)dppu<XOR1>((unsigned)d); d += (int)dppu<XOR2>((unsigned)d); d += (int)dppu<ROR4>((unsigned)d); d += (int)dppu<ROR8>((unsigned)d);
      d = __builtin_amdgcn_readlane(d, 0) + __builtin_amdgcn_readlane(d, 16) + __builtin_amdgcn_readlane(d, 32) + __builtin_amdgcn_readlane(d, 48);
      if (lane == 0) sidew[0] = (d > 0 ? 1u : 0u) ^ (unsigned)(kvh >> 2);
    }
  }
  wg_barrier();
  const int nb = __builtin_amdgcn_readfirstlane(sidew[0]);

  const int h = r >> 3, jj = r & 7;
  u32x4 QB[4];
#pragma unroll
  for (int u = 0; u < 4; ++u) QB[u] = *(const u32x4*)((const char*)qs + h * 512 + u * 128 + (r & 1) * 64 + g * 16);
  float sc[8];
#pragma unroll
  for (int st = 0; st < 4; ++st) {
    f32x4 S = {0.f, 0.f, 0.f, 0.f};
#pragma unroll
    for (int u = 0; u < 4; ++u) S = mfma16(K[st * 4 + u], QB[u], S);
    const float a = (r & 1) ? S[1] : S[0], c = (r & 1) ? S[3] : S[2];
    const unsigned sp = cvtpk(a + dpp<XOR1>(a), c + dpp<XOR1>(c));
    sc[2 * st] = bflo(sp);
    sc[2 * st + 1] = bfhi(sp);
  }
  float m = fmaxf(fmaxf(fmaxf(sc[0], sc[1]), fmaxf(sc[2], sc[3])), fmaxf(fmaxf(sc[4], sc[5]), fmaxf(sc[6], sc[7])));
  m = x16_32_max(m);
  if ((lane & 0x37) == 0) mx[w * 2 + h] = m;
  wg_barrier();
  float M;
  {
    const float* p = mx + h;
    M = fmaxf(fmaxf(fmaxf(p[0], p[2]), fmaxf(p[4], p[6])), fmaxf(fmaxf(p[8], p[10]), fmaxf(p[12], p[14])));
  }
  float pv[8], l = 0.f;
#pragma unroll
  for (int i = 0; i < 8; ++i) { pv[i] = __builtin_amdgcn_exp2f((sc[i] - M) * C2); l += pv[i]; }
  l = x16_32_sum(l);
  if ((lane & 0x37) == 0) ls[w * 2 + h] = l;
  unsigned msk[4];
#pragma unroll
  for (int i = 0; i < 4; ++i) msk[i] = ((jj >> 1) == i) ? ((jj & 1) ? 0xffff0000u : 0x0000ffffu) : 0u;
  LOADV(2); LOADV(3);
  __builtin_amdgcn_sched_barrier(0);
  wg_barrier();
  float invl, lse;
  int pgoff, ygoff;
  {
    const int hp2 = (t >> 7) & 1;
    float L = 0.f, MM = mx[hp2];
#pragma unroll
    for (int i = 0; i < 8; ++i) { L += ls[i * 2 + hp2]; MM = fmaxf(MM, mx[i * 2 + hp2]); }
    invl = __builtin_amdgcn_rcpf(L);
    lse = fmaf(MM, C2, __builtin_amdgcn_logf(L));
    const int c = (t >> 3) & 31;
    pgoff = HP_A + (kvh & 3) * 262144 + ((c >> 1) * 4 + nb * 2 + (c & 1)) * 4096 + s * 128 + (t & 7) * 16;
    const int gi = w * 64 + 4 * r + (g & 1) * 2 + (g >> 1), cb = gi >> 3, mb = (cb >> 1) * 8 + kvh;
    ygoff = (gi < 224) ? HP_B + ((mb >> 1) * 4 + (cb & 1) * 2 + (mb & 1)) * 4096 + s * 128 + (gi & 7) * 16 : OOB;
  }
  asm volatile("" : "+v"(invl), "+v"(lse), "+v"(pgoff), "+v"(ygoff));

  f32x4 acc0 = {0.f, 0.f, 0.f, 0.f}, acc1 = {0.f, 0.f, 0.f, 0.f};
#pragma unroll
  for (int st = 0; st < 4; ++st)
#pragma unroll
    for (int i = 0; i < 2; ++i) {
      const unsigned pp = cvtpk(pv[2 * st + i], pv[2 * st + i]);
      const u32x4 A = {pp & msk[0], pp & msk[1], pp & msk[2], pp & msk[3]};
      acc0 = mfma16(A, V[st * 4 + i * 2], acc0);
      acc1 = mfma16(A, V[st * 4 + i * 2 + 1], acc1);
    }
  *(f32x4*)(red + w * 512 + (g >> 1) * 256 + 8 * r + (g & 1) * 4) = acc0;
  *(f32x4*)(red + w * 512 + (g >> 1) * 256 + 128 + 8 * r + (g & 1) * 4) = acc1;
  wg_barrier();
  if (w < 4) {
    f32x2 pr[8];
#pragma unroll
    for (int i = 0; i < 8; ++i) pr[i] = *(const f32x2*)(red + i * 512 + 2 * t);
    const f32x2 sum = ((pr[0] + pr[1]) + (pr[2] + pr[3])) + ((pr[4] + pr[5]) + (pr[6] + pr[7]));
    const u32x4 gr = {__float_as_uint(sum[0] * invl), T, __float_as_uint(sum[1] * invl), __float_as_uint(lse)};
    ST128(gr, rws, pgoff);
    __builtin_amdgcn_sched_barrier(0);
    const int wooff = (4 * g) * (DM * 2) + r * 16 + w * 256;
#pragma unroll
    for (int st = 0; st < 4; ++st) WO[st] = LD128(rwo, wooff + st * (DM * 2), wg * 16 * (DM * 2), NT);
  }
  if (w == 4) {
    const int pbase = HP_A + (kvh & 3) * 262144 + ((s >> 1) * 4 + nb * 2 + (s & 1)) * 4096 + lane * 16;
    u32x4 G[4];
    unsigned spins = 0;
    wait_ticks(DELAY_A);
#pragma unroll 1
    while (true) {
#pragma unroll
      for (int i = 0; i < 4; ++i) G[i] = LD128(rws, pbase + i * 1024, 0, SC1);
      unsigned bad = 0;
#pragma unroll
      for (int i = 0; i < 4; ++i) bad |= (G[i][1] ^ T) | (G[i][3] ^ dppu<XOR1>(G[i][3])) | (G[i][3] ^ dppu<XOR2>(G[i][3])) | (G[i][3] ^ dppu<XOR7>(G[i][3]));
      asm volatile("" : "+v"(bad));
      if (__builtin_amdgcn_ballot_w64(bad != 0u) == 0ull || ++spins > SPIN_CAP) break;
      __builtin_amdgcn_s_sleep(1);
      asm volatile("" ::: "memory");
    }
    float Mg = fmaxf(fmaxf(__uint_as_float(G[0][3]), __uint_as_float(G[1][3])), fmaxf(__uint_as_float(G[2][3]), __uint_as_float(G[3][3])));
    Mg = fmaxf(Mg, dpp<ROR8>(Mg)); Mg = x16_32_max(Mg);
    float L = 0.f, o0 = 0.f, o1 = 0.f;
#pragma unroll
    for (int i = 0; i < 4; ++i) {
      const float wt = __builtin_amdgcn_exp2f(__uint_as_float(G[i][3]) - Mg);
      L += wt; o0 = fmaf(wt, __uint_as_float(G[i][0]), o0); o1 = fmaf(wt, __uint_as_float(G[i][2]), o1);
    }
    L += dpp<ROR8>(L); o0 += dpp<ROR8>(o0); o1 += dpp<ROR8>(o1);
    L = x16_32_sum(L); o0 = x16_32_sum(o0); o1 = x16_32_sum(o1);
    const float inv = __builtin_amdgcn_rcpf(L);
    o0 *= inv; o1 *= inv;
    if (lane < 8) *(u32x2*)(odup + 2 * lane) = u32x2{cvtpk(o0, o0), cvtpk(o1, o1)};
  }
  wg_barrier();
  if (w < 4) {
    const u32x4 ov = *(const u32x4*)(odup + 4 * g);
    f32x4 yp[4];
#pragma unroll
    for (int st = 0; st < 4; ++st) {
      const u32x4 A = {ov[st] & msk[0], ov[st] & msk[1], ov[st] & msk[2], ov[st] & msk[3]};
      yp[st] = mfma16(A, WO[st], f32x4{0.f, 0.f, 0.f, 0.f});
    }
    const f32x4 ya = (yp[0] + yp[1]) + (yp[2] + yp[3]);
    const float y0 = (g >> 1) ? ya[2] : ya[0], y1 = (g >> 1) ? ya[3] : ya[1];
    const u32x4 gr = {__float_as_uint(y0), T, __float_as_uint(y1), T};
    ST128(gr, rws, ygoff);
  }
  const int kred = s * 4 + (kvh & 3);
  if (kred < 14) {
    const int c = kred * 2 + nb, mb = kred * 8 + w;
    const int ybase = HP_B + ((mb >> 1) * 4 + nb * 2 + (mb & 1)) * 4096 + lane * 16;
    u32x4 G[4];
    unsigned spins = 0;
    wait_ticks(DELAY_B);
#pragma unroll 1
    while (true) {
#pragma unroll
      for (int i = 0; i < 4; ++i) G[i] = LD128(rws, ybase + i * 1024, 0, SC1);
      unsigned bad = 0;
#pragma unroll
      for (int i = 0; i < 4; ++i) bad |= (G[i][1] ^ T) | (G[i][3] ^ T);
      asm volatile("" : "+v"(bad));
      if (__builtin_amdgcn_ballot_w64(bad != 0u) == 0ull || ++spins > SPIN_CAP) break;
      __builtin_amdgcn_s_sleep(1);
      asm volatile("" ::: "memory");
    }
    float y0 = (__uint_as_float(G[0][0]) + __uint_as_float(G[1][0])) + (__uint_as_float(G[2][0]) + __uint_as_float(G[3][0]));
    float y1 = (__uint_as_float(G[0][2]) + __uint_as_float(G[1][2])) + (__uint_as_float(G[2][2]) + __uint_as_float(G[3][2]));
    y0 += dpp<ROR8>(y0); y1 += dpp<ROR8>(y1);
    {
      auto sw = __builtin_amdgcn_permlane16_swap(__float_as_uint(y0), __float_as_uint(y1), false, false);
      y0 = x32_sum(__uint_as_float(sw[0]) + __uint_as_float(sw[1]));
    }
    if ((lane & 40) == 0) fin[w * 16 + (lane & 7) * 2 + (lane >> 4)] = y0;
    wg_barrier();
    if (t < 8) {
      f32x2 pr[8];
#pragma unroll
      for (int i = 0; i < 8; ++i) pr[i] = *(const f32x2*)(fin + i * 16 + t * 2);
      const f32x2 sum = ((pr[0] + pr[1]) + (pr[2] + pr[3])) + ((pr[4] + pr[5]) + (pr[6] + pr[7]));
      __hip_atomic_store((GAS unsigned*)out + c * 8 + t, cvtpk(sum[0], sum[1]), __ATOMIC_RELAXED, __HIP_MEMORY_SCOPE_AGENT);
      if (wg == 0 && t == 0) __hip_atomic_store((GAS unsigned*)ws, T, __ATOMIC_RELAXED, __HIP_MEMORY_SCOPE_AGENT);
    }
  }
}

extern "C" void kernel_launch(void* const* d_in, const int* in_sizes, int n_in,
                              void* d_out, int out_size, void* d_ws, size_t ws_size,
                              hipStream_t stream) {
  gqa_decode_fused<<<256, NTHR, 0, stream>>>((const u16*)d_in[0], (const u16*)d_in[1], (const u16*)d_in[2], (const u16*)d_in[3],
                                             (const u16*)d_in[4], (u16*)d_out, (char*)d_ws);
}
```

```cpp
#include <hip/hip_runtime.h>
#include <hip/hip_bf16.h>
#include <stdint.h>

typedef unsigned u32x4 __attribute__((ext_vector_type(4)));
typedef unsigned u32x2 __attribute__((ext_vector_type(2)));
typedef float f32x4 __attribute__((ext_vector_type(4)));
typedef float f32x2 __attribute__((ext_vector_type(2)));
typedef __bf16 bf16x8 __attribute__((ext_vector_type(8)));
typedef __bf16 bf16x2 __attribute__((ext_vector_type(2)));
typedef unsigned short u16;
#define GAS __attribute__((address_space(1)))

constexpr int DM = 448, SK = 8192;
constexpr int NW = 8, NTHR = NW * 64;
constexpr float C2 = 0.0625f * 1.4426950408889634f;
constexpr int NT = 2, SC1 = 16;
constexpr int OOB = 0x7ff00000;
constexpr unsigned SPIN_CAP = 1u << 18;
constexpr int DELAY_A = 70, DELAY_B = 30;

constexpr int HP_A = 0;
constexpr int HP_B = 1 << 20;
constexpr int HP_PROBE = HP_B + 224 * 4096;
constexpr int WS_QG = 2 << 20;
constexpr int QG_STRIDE = 512;
constexpr int WS_PR = (2 << 20) + 262144;
constexpr int WS_E = (2 << 20) + 270336;

__device__ __forceinline__ unsigned cvtpk(float lo, float hi) { f32x2 v = {lo, hi}; return __builtin_bit_cast(unsigned, __builtin_convertvector(v, bf16x2)); }
__device__ __forceinline__ float bflo(unsigned u) { return __uint_as_float(u << 16); }
__device__ __forceinline__ float bfhi(unsigned u) { return __uint_as_float(u & 0xffff0000u); }
__device__ __forceinline__ f32x4 mfma16(u32x4 a, u32x4 b, f32x4 c) {
  return __builtin_amdgcn_mfma_f32_16x16x32_bf16(__builtin_bit_cast(bf16x8, a), __builtin_bit_cast(bf16x8, b), c, 0, 0, 0);
}
template <int CTRL> __device__ __forceinline__ float dpp(float x) {
  return __builtin_bit_cast(float, __builtin_amdgcn_mov_dpp(__builtin_bit_cast(int, x), CTRL, 0xf, 0xf, true));
}
template <int CTRL> __device__ __forceinline__ unsigned dppu(unsigned x) {
  return (unsigned)__builtin_amdgcn_mov_dpp((int)x, CTRL, 0xf, 0xf, true);
}
constexpr int XOR1 = 0xB1, XOR2 = 0x4E, XOR7 = 0x141, ROR8 = 0x128, ROR4 = 0x124;
__device__ __forceinline__ float x32_sum(float x) {
  auto t = __builtin_amdgcn_permlane32_swap(__float_as_uint(x), __float_as_uint(x), false, false);
  return __uint_as_float(t[0]) + __uint_as_float(t[1]);
}
__device__ __forceinline__ float x16_32_sum(float x) {
  auto s = __builtin_amdgcn_permlane16_swap(__float_as_uint(x), __float_as_uint(x), false, false);
  return x32_sum(__uint_as_float(s[0]) + __uint_as_float(s[1]));
}
__device__ __forceinline__ float x16_32_max(float x) {
  auto s = __builtin_amdgcn_permlane16_swap(__float_as_uint(x), __float_as_uint(x), false, false);
  x = fmaxf(__uint_as_float(s[0]), __uint_as_float(s[1]));
  auto t = __builtin_amdgcn_permlane32_swap(__float_as_uint(x), __float_as_uint(x), false, false);
  return fmaxf(__uint_as_float(t[0]), __uint_as_float(t[1]));
}
__device__ __forceinline__ void wg_barrier() { asm volatile("s_waitcnt lgkmcnt(0)\n\ts_barrier" ::: "memory"); }
__device__ __forceinline__ void wait_ticks(int n) {
  const unsigned t0 = (unsigned)__builtin_amdgcn_s_memrealtime();
  while ((int)((unsigned)__builtin_amdgcn_s_memrealtime() - t0) < n) __builtin_amdgcn_s_sleep(1);
}
#define LD128(rs, voff, soff, aux) __builtin_amdgcn_raw_buffer_load_b128(rs, voff, soff, aux)
#define ST128(v, rs, voff) __builtin_amdgcn_raw_buffer_store_b128(v, rs, voff, 0, SC1)

__global__ void __launch_bounds__(NTHR, 2)
gqa_decode_fused(const u16* __restrict__ x, const u16* __restrict__ kc, const u16* __restrict__ vc, const u16* __restrict__ wq,
                 const u16* __restrict__ wo, u16* out, char* ws) {
  __shared__ __attribute__((aligned(16))) float lds[4096 + 256 + 512 + 16 + 16 + 16 + 128 + 4 + 16000];
  float* red = lds;
  unsigned* qs = (unsigned*)(lds + 4096);
  float* qpart = lds + 4096 + 256;
  float* mx = qpart + 512;
  float* ls = mx + 16;
  unsigned* odup = (unsigned*)(ls + 16);
  float* fin = ls + 32;
  unsigned* sidew = (unsigned*)(fin + 128);

  const int kvh = blockIdx.x & 7, s = blockIdx.x >> 3;
  const int wg = kvh * 32 + s;
  const int t = threadIdx.x, lane = t & 63, w = __builtin_amdgcn_readfirstlane(t >> 6), g = lane >> 4, r = lane & 15;

  char* hp = (char*)(((size_t)ws + 0x1fffff) & ~(size_t)0x1fffff);
  const __amdgpu_buffer_rsrc_t rws = __builtin_amdgcn_make_buffer_rsrc(hp, 0, 4u << 20, 0x00020000);
  const size_t kvoff = ((size_t)kvh * SK + s * 256) * 512;
  const __amdgpu_buffer_rsrc_t rk = __builtin_amdgcn_make_buffer_rsrc((char*)kc + kvoff, 0, 131072, 0x00020000);
  const __amdgpu_buffer_rsrc_t rv = __builtin_amdgcn_make_buffer_rsrc((char*)vc + kvoff, 0, 131072, 0x00020000);
  const __amdgpu_buffer_rsrc_t rwq = __builtin_amdgcn_make_buffer_rsrc((void*)wq, 0, DM * 4096 * 2, 0x00020000);
  const __amdgpu_buffer_rsrc_t rwo = __builtin_amdgcn_make_buffer_rsrc((void*)wo, 0, DM * 4096 * 2, 0x00020000);
  const __amdgpu_buffer_rsrc_t rx = __builtin_amdgcn_make_buffer_rsrc((void*)x, 0, DM * 2, 0x00020000);

  {
    const int pl = kvh * 16 + (s >> 1);
    const int pbase = HP_PROBE + (pl >> 6) * 32768 + (s & 1) * 65536 + (pl & 63) * 128;
    __builtin_amdgcn_raw_buffer_store_b32(0u, rws, (t == 0) ? pbase + (s & 1) * 8192 : OOB, 0, SC1);
    __builtin_amdgcn_raw_buffer_store_b32(0u, rws, (t == 0) ? pbase + ((s & 1) ^ 1) * 8192 : OOB, 0, SC1);
  }
  const unsigned Eraw = __hip_atomic_load((GAS unsigned*)(hp + WS_E), __ATOMIC_RELAXED, __HIP_MEMORY_SCOPE_AGENT);

  u32x4 WQ[2]; unsigned short xv[2];
  {
    const int row0 = 112 * (s & 3) + 16 * (w - 1) + (lane >> 3);
    const int oob = (w > 0) ? 0 : 0x40000000;
#pragma unroll
    for (int i = 0; i < 2; ++i) {
      WQ[i] = LD128(rwq, (row0 + 8 * i) * 8192 + (lane & 7) * 16 + oob, (kvh * 8 + (s >> 2)) * 128, NT);
      xv[i] = __builtin_amdgcn_raw_buffer_load_b16(rx, (row0 + 8 * i) * 2 + oob, 0, 0);
    }
  }
  wg_barrier();
  unsigned plat = 0;
  if (w == 0) {
    unsigned long long pc1, pc2;
    asm volatile("s_waitcnt vmcnt(6)\n\ts_memtime %0\n\ts_waitcnt lgkmcnt(0) vmcnt(5)\n\ts_memtime %1\n\ts_waitcnt lgkmcnt(0)" : "=&s"(pc1), "=&s"(pc2) :: "memory");
    plat = min((unsigned)(pc2 - pc1), 2000u);
  }

  u32x4 K[16], V[16], WO[4], QG[2];
  const int koff = (r >> 1) * 512 + (r & 1) * 64 + g * 16;
  const int voff = g * 1024 + r * 16;
#define LOADK(st) _Pragma("unroll") for (int u = 0; u < 4; ++u) K[(st) * 4 + u] = LD128(rk, koff + u * 128, ((st) * 8 + w) * 4096, NT)
#define LOADV(st) _Pragma("unroll") for (int u = 0; u < 4; ++u) V[(st) * 4 + u] = LD128(rv, voff + (u >> 1) * 512 + (u & 1) * 256, ((st) * 8 + w) * 4096, NT)
  LOADK(0); LOADK(1);
  __builtin_amdgcn_sched_barrier(0);
  const unsigned T = __builtin_amdgcn_readfirstlane(Eraw) + 1u;
  {
    float a[8];
#pragma unroll
    for (int j = 0; j < 8; ++j) a[j] = 0.f;
#pragma unroll
    for (int i = 0; i < 2; ++i) {
      const float xf = bflo(xv[i]);
#pragma unroll
      for (int j = 0; j < 4; ++j) { a[2 * j] = fmaf(xf, bflo(WQ[i][j]), a[2 * j]); a[2 * j + 1] = fmaf(xf, bfhi(WQ[i][j]), a[2 * j + 1]); }
    }
#pragma unroll
    for (int j = 0; j < 8; ++j) { float v = a[j]; v += dpp<ROR8>(v); a[j] = x16_32_sum(v); }
    if (lane < 8) {
      *(f32x4*)(qpart + w * 64 + lane * 8) = f32x4{a[0], a[1], a[2], a[3]};
      *(f32x4*)(qpart + w * 64 + lane * 8 + 4) = f32x4{a[4], a[5], a[6], a[7]};
    }
  }
  wg_barrier();
  if (w == 0) {
    if (lane < 32) {
      f32x2 sum = *(const f32x2*)(qpart + 64 + lane * 2);
#pragma unroll
      for (int i = 2; i < 8; ++i) sum += *(const f32x2*)(qpart + i * 64 + lane * 2);
      const u32x4 gr = {__float_as_uint(sum[0]), T, __float_as_uint(sum[1]), T};
      ST128(gr, rws, WS_QG + wg * QG_STRIDE + lane * 16);
    } else if (lane == 32) {
      const u32x4 gr = {plat, T, plat, T};
      ST128(gr, rws, WS_PR + wg * 16);
    }
  }
  wg_barrier();
  LOADK(2); LOADK(3); LOADV(0);
  wg_barrier();
  const int qgoff = WS_QG + (kvh * 32 + w * 4 + (lane >> 5)) * QG_STRIDE + (lane & 31) * 16;
  const int proff = (w == 0) ? WS_PR + ((kvh & 3) * 32 + (lane >> 5) * 128 + (lane & 31)) * 16 : OOB;
  const unsigned Tpr = (w == 0) ? T : 0u;
#pragma unroll
  for (int i = 0; i < 2; ++i) QG[i] = LD128(rws, qgoff + i * 2 * QG_STRIDE, 0, SC1);
  u32x4 PR = LD128(rws, proff, 0, SC1);
  wg_barrier();
  LOADV(1);
  __builtin_amdgcn_sched_barrier(0);
  if (!__all(((QG[0][1] ^ T) | (QG[0][3] ^ T) | (QG[1][1] ^ T) | (QG[1][3] ^ T) | (PR[1] ^ Tpr) | (PR[3] ^ Tpr)) == 0u)) {
#pragma unroll 1
    for (unsigned spins = 0; spins < SPIN_CAP; ++spins) {
      asm volatile("" ::: "memory");
#pragma unroll
      for (int i = 0; i < 2; ++i) QG[i] = LD128(rws, qgoff + i * 2 * QG_STRIDE, 0, SC1);
      PR = LD128(rws, proff, 0, SC1);
      if (__all(((QG[0][1] ^ T) | (QG[0][3] ^ T) | (QG[1][1] ^ T) | (QG[1][3] ^ T) | (PR[1] ^ Tpr) | (PR[3] ^ Tpr)) == 0u)) break;
      __builtin_amdgcn_s_sleep(1);
    }
  }
  {
    const float q0 = x32_sum(__uint_as_float(QG[0][0]) + __uint_as_float(QG[1][0]));
    const float q1 = x32_sum(__uint_as_float(QG[0][2]) + __uint_as_float(QG[1][2]));
    if (lane < 32) qs[w * 32 + lane] = cvtpk(q0, q1);
    if (w == 0) {
      int d = (((lane ^ (lane >> 5)) & 1) ? 1 : -1) * (int)PR[0];
      d += (int)dppu<XOR1>((unsigned)d); d += (int)dppu<XOR2>((unsigned)d); d += (int)dppu<ROR4>((unsigned)d); d += (int)dppu<ROR8>((unsigned)d);
      d = __builtin_amdgcn_readlane(d, 0) + __builtin_amdgcn_readlane(d, 16) + __builtin_amdgcn_readlane(d, 32) + __builtin_amdgcn_readlane(d, 48);
      if (lane == 0) sidew[0] = (d > 0 ? 1u : 0u) ^ (unsigned)(kvh >> 2);
    }
  }
  wg_barrier();
  const int nb = __builtin_amdgcn_readfirstlane(sidew[0]);

  const int h = r >> 3, jj = r & 7;
  u32x4 QB[4];
#pragma unroll
  for (int u = 0; u < 4; ++u) QB[u] = *(const u32x4*)((const char*)qs + h * 512 + u * 128 + (r & 1) * 64 + g * 16);
  float sc[8];
#pragma unroll
  for (int st = 0; st < 4; ++st) {
    f32x4 S = {0.f, 0.f, 0.f, 0.f};
#pragma unroll
    for (int u = 0; u < 4; ++u) S = mfma16(K[st * 4 + u], QB[u], S);
    const float a = (r & 1) ? S[1] : S[0], c = (r & 1) ? S[3] : S[2];
    const unsigned sp = cvtpk(a + dpp<XOR1>(a), c + dpp<XOR1>(c));
    sc[2 * st] = bflo(sp);
    sc[2 * st + 1] = bfhi(sp);
  }
  float m = fmaxf(fmaxf(fmaxf(sc[0], sc[1]), fmaxf(sc[2], sc[3])), fmaxf(fmaxf(sc[4], sc[5]), fmaxf(sc[6], sc[7])));
  m = x16_32_max(m);
  if ((lane & 0x37) == 0) mx[w * 2 + h] = m;
  wg_barrier();
  float M;
  {
    const float* p = mx + h;
    M = fmaxf(fmaxf(fmaxf(p[0], p[2]), fmaxf(p[4], p[6])), fmaxf(fmaxf(p[8], p[10]), fmaxf(p[12], p[14])));
  }
  float pv[8], l = 0.f;
#pragma unroll
  for (int i = 0; i < 8; ++i) { pv[i] = __builtin_amdgcn_exp2f((sc[i] - M) * C2); l += pv[i]; }
  l = x16_32_sum(l);
  if ((lane & 0x37) == 0) ls[w * 2 + h] = l;
  unsigned msk[4];
#pragma unroll
  for (int i = 0; i < 4; ++i) msk[i] = ((jj >> 1) == i) ? ((jj & 1) ? 0xffff0000u : 0x0000ffffu) : 0u;
  LOADV(2); LOADV(3);
  __builtin_amdgcn_sched_barrier(0);
  wg_barrier();
  float invl, lse;
  int pgoff, ygoff;
  {
    const int hp2 = (t >> 7) & 1;
    float L = 0.f, MM = mx[hp2];
#pragma unroll
    for (int i = 0; i < 8; ++i) { L += ls[i * 2 + hp2]; MM = fmaxf(MM, mx[i * 2 + hp2]); }
    invl = __builtin_amdgcn_rcpf(L);
    lse = fmaf(MM, C2, __builtin_amdgcn_logf(L));
    const int c = (t >> 3) & 31;
    pgoff = HP_A + (kvh & 3) * 262144 + ((c >> 1) * 4 + nb * 2 + (c & 1)) * 4096 + s * 128 + (t & 7) * 16;
    const int gi = w * 64 + 4 * r + (g & 1) * 2 + (g >> 1), cb = gi >> 3, mb = (cb >> 1) * 8 + kvh;
    ygoff = (gi < 224) ? HP_B + ((mb >> 1) * 4 + (cb & 1) * 2 + (mb & 1)) * 4096 + s * 128 + (gi & 7) * 16 : OOB;
  }
  asm volatile("" : "+v"(invl), "+v"(lse), "+v"(pgoff), "+v"(ygoff));

  f32x4 acc0 = {0.f, 0.f, 0.f, 0.f}, acc1 = {0.f, 0.f, 0.f, 0.f};
#pragma unroll
  for (int st = 0; st < 4; ++st)
#pragma unroll
    for (int i = 0; i < 2; ++i) {
      const unsigned pp = cvtpk(pv[2 * st + i], pv[2 * st + i]);
      const u32x4 A = {pp & msk[0], pp & msk[1], pp & msk[2], pp & msk[3]};
      acc0 = mfma16(A, V[st * 4 + i * 2], acc0);
      acc1 = mfma16(A, V[st * 4 + i * 2 + 1], acc1);
    }
  *(f32x4*)(red + w * 512 + (g >> 1) * 256 + 8 * r + (g & 1) * 4) = acc0;
  *(f32x4*)(red + w * 512 + (g >> 1) * 256 + 128 + 8 * r + (g & 1) * 4) = acc1;
  wg_barrier();
  if (w < 4) {
    f32x2 pr[8];
#pragma unroll
    for (int i = 0; i < 8; ++i) pr[i] = *(const f32x2*)(red + i * 512 + 2 * t);
    const f32x2 sum = ((pr[0] + pr[1]) + (pr[2] + pr[3])) + ((pr[4] + pr[5]) + (pr[6] + pr[7]));
    const u32x4 gr = {__float_as_uint(sum[0] * invl), T, __float_as_uint(sum[1] * invl), __float_as_uint(lse)};
    ST128(gr, rws, pgoff);
    __builtin_amdgcn_sched_barrier(0);
    const int wooff = (4 * g) * (DM * 2) + r * 16 + w * 256;
#pragma unroll
    for (int st = 0; st < 4; ++st) WO[st] = LD128(rwo, wooff + st * (DM * 2), wg * 16 * (DM * 2), NT);
  }
  if (w == 4) {
    const int pbase = HP_A + (kvh & 3) * 262144 + ((s >> 1) * 4 + nb * 2 + (s & 1)) * 4096 + lane * 16;
    u32x4 G[4];
    unsigned spins = 0;
    wait_ticks(DELAY_A);
#pragma unroll 1
    while (true) {
#pragma unroll
      for (int i = 0; i < 4; ++i) G[i] = LD128(rws, pbase + i * 1024, 0, SC1);
      unsigned bad = 0;
#pragma unroll
      for (int i = 0; i < 4; ++i) bad |= (G[i][1] ^ T) | (G[i][3] ^ dppu<XOR1>(G[i][3])) | (G[i][3] ^ dppu<XOR2>(G[i][3])) | (G[i][3] ^ dppu<XOR7>(G[i][3]));
      asm volatile("" : "+v"(bad));
      if (__builtin_amdgcn_ballot_w64(bad != 0u) == 0ull || ++spins > SPIN_CAP) break;
      __builtin_amdgcn_s_sleep(1);
      asm volatile("" ::: "memory");
    }
    float Mg = fmaxf(fmaxf(__uint_as_float(G[0][3]), __uint_as_float(G[1][3])), fmaxf(__uint_as_float(G[2][3]), __uint_as_float(G[3][3])));
    Mg = fmaxf(Mg, dpp<ROR8>(Mg)); Mg = x16_32_max(Mg);
    float L = 0.f, o0 = 0.f, o1 = 0.f;
#pragma unroll
    for (int i = 0; i < 4; ++i) {
      const float wt = __builtin_amdgcn_exp2f(__uint_as_float(G[i][3]) - Mg);
      L += wt; o0 = fmaf(wt, __uint_as_float(G[i][0]), o0); o1 = fmaf(wt, __uint_as_float(G[i][2]), o1);
    }
    L += dpp<ROR8>(L); o0 += dpp<ROR8>(o0); o1 += dpp<ROR8>(o1);
    L = x16_32_sum(L); o0 = x16_32_sum(o0); o1 = x16_32_sum(o1);
    const float inv = __builtin_amdgcn_rcpf(L);
    o0 *= inv; o1 *= inv;
    if (lane < 8) *(u32x2*)(odup + 2 * lane) = u32x2{cvtpk(o0, o0), cvtpk(o1, o1)};
  }
  wg_barrier();
  if (w < 4) {
    const u32x4 ov = *(const u32x4*)(odup + 4 * g);
    f32x4 yp[4];
#pragma unroll
    for (int st = 0; st < 4; ++st) {
      const u32x4 A = {ov[st] & msk[0], ov[st] & msk[1], ov[st] & msk[2], ov[st] & msk[3]};
      yp[st] = mfma16(A, WO[st], f32x4{0.f, 0.f, 0.f, 0.f});
    }
    const f32x4 ya = (yp[0] + yp[1]) + (yp[2] + yp[3]);
    const float y0 = (g >> 1) ? ya[2] : ya[0], y1 = (g >> 1) ? ya[3] : ya[1];
    const u32x4 gr = {__float_as_uint(y0), T, __float_as_uint(y1), T};
    ST128(gr, rws, ygoff);
  }
  const int kred = s * 4 + (kvh & 3);
  if (kred < 14) {
    const int c = kred * 2 + nb, mb = kred * 8 + w;
    const int ybase = HP_B + ((mb >> 1) * 4 + nb * 2 + (mb & 1)) * 4096 + lane * 16;
    u32x4 G[4];
    unsigned spins = 0;
    wait_ticks(DELAY_B);
#pragma unroll 1
    while (true) {
#pragma unroll
      for (int i = 0; i < 4; ++i) G[i] = LD128(rws, ybase + i * 1024, 0, SC1);
      unsigned bad = 0;
#pragma unroll
      for (int i = 0; i < 4; ++i) bad |= (G[i][1] ^ T) | (G[i][3] ^ T);
      asm volatile("" : "+v"(bad));
      if (__builtin_amdgcn_ballot_w64(bad != 0u) == 0ull || ++spins > SPIN_CAP) break;
      __builtin_amdgcn_s_sleep(1);
      asm volatile("" ::: "memory");
    }
    float y0 = (__uint_as_float(G[0][0]) + __uint_as_float(G[1][0])) + (__uint_as_float(G[2][0]) + __uint_as_float(G[3][0]));
    float y1 = (__uint_as_float(G[0][2]) + __uint_as_float(G[1][2])) + (__uint_as_float(G[2][2]) + __uint_as_float(G[3][2]));
    y0 += dpp<ROR8>(y0); y1 += dpp<ROR8>(y1);
    {
      auto sw = __builtin_amdgcn_permlane16_swap(__float_as_uint(y0), __float_as_uint(y1), false, false);
      y0 = x32_sum(__uint_as_float(sw[0]) + __uint_as_float(sw[1]));
    }
    if ((lane & 40) == 0) fin[w * 16 + (lane & 7) * 2 + (lane >> 4)] = y0;
    wg_barrier();
    if (t < 8) {
      f32x2 pr[8];
#pragma unroll
      for (int i = 0; i < 8; ++i) pr[i] = *(const f32x2*)(fin + i * 16 + t * 2);
      const f32x2 sum = ((pr[0] + pr[1]) + (pr[2] + pr[3])) + ((pr[4] + pr[5]) + (pr[6] + pr[7]));
      __hip_atomic_store((GAS unsigned*)out + c * 8 + t, cvtpk(sum[0], sum[1]), __ATOMIC_RELAXED, __HIP_MEMORY_SCOPE_AGENT);
      if (wg == 0 && t == 0) __hip_atomic_store((GAS unsigned*)(hp + WS_E), T, __ATOMIC_RELAXED, __HIP_MEMORY_SCOPE_AGENT);
    }
  }
}

extern "C" void kernel_launch(void* const* d_in, const int* in_sizes, int n_in,
                              void* d_out, int out_size, void* d_ws, size_t ws_size,
                              hipStream_t stream) {
  gqa_decode_fused<<<256, NTHR, 0, stream>>>((const u16*)d_in[0], (const u16*)d_in[1], (const u16*)d_in[2], (const u16*)d_in[3],
                                             (const u16*)d_in[4], (u16*)d_out, (char*)d_ws);
}
```
